```python
import torch

def _softmax(x, axis=-1):
    return torch.softmax(x, dim=axis)
def _gelu(x):
    return torch.nn.functional.gelu(x, approximate="tanh")
def _silu(x):
    return torch.nn.functional.silu(x)
def _sigmoid(x):
    return torch.sigmoid(x)
def _layernorm(x, axis=-1, eps=1e-5):
    m = x.mean(dim=axis, keepdim=True)
    v = x.var(dim=axis, keepdim=True, unbiased=False)
    return (x - m) / torch.sqrt(v + eps)
def _scatter_add(tgt, idx, src, axis=0):
    return tgt.clone().index_add_(axis, idx, src)
def _segment_sum(src, seg, n):
    out = torch.zeros((n,) + src.shape[1:], dtype=src.dtype)
    return out.index_add_(0, seg, src)
def _pad(x, pw):
    flat = tuple(v for pair in reversed(pw) for v in pair)
    return torch.nn.functional.pad(x, flat)

kernel_name = 'standalone__glu_gated_geglu__prompt'

def setup_inputs():

    _w = (lambda a: (lambda t: t.to(torch.bfloat16) if t.is_floating_point() else t)(torch.as_tensor(a)))
    B, S, Dm, Df = 1, 2048, 512, 1376
    return {
        "x": _w((lambda *s: torch.randn(*s, dtype=torch.bfloat16) * 0.05)(B, S, Dm)),
        "w_gate": _w((lambda *s: torch.randn(*s, dtype=torch.bfloat16) * 0.05)(Dm, Df) * (Dm ** -0.5)),
        "w_up": _w((lambda *s: torch.randn(*s, dtype=torch.bfloat16) * 0.05)(Dm, Df) * (Dm ** -0.5)),
        "w_down": _w((lambda *s: torch.randn(*s, dtype=torch.bfloat16) * 0.05)(Df, Dm) * (Df ** -0.5)),
    }

def reference(x, w_gate, w_up, w_down):
    g = torch.einsum("bsd,df->bsf", x, w_gate)
    u = torch.einsum("bsd,df->bsf", x, w_up)
    h = _gelu(g) * u
    return torch.einsum("bsf,fd->bsd", h, w_down)
```

```cpp
#include <hip/hip_runtime.h>
#include <hip/hip_bf16.h>
typedef short bf16x8 __attribute__((ext_vector_type(8)));
typedef float f32x16 __attribute__((ext_vector_type(16)));
typedef unsigned short u16;

constexpr int S = 2048, DM = 512, DF = 1376;

__device__ __forceinline__ float bf2f(u16 v) { return __uint_as_float((unsigned)v << 16); }
__device__ __forceinline__ u16 f2bf(float f) { __hip_bfloat16 b = __float2bfloat16(f); return *(u16*)&b; }
__device__ __forceinline__ float rbf(float f) { return bf2f(f2bf(f)); }

__global__ __launch_bounds__(64) void k1(const u16* x, const u16* wg, const u16* wu, u16* h) {
  int lane = threadIdx.x, r = lane & 31, hh = lane >> 5;
  int f0 = blockIdx.x * 32, m0 = blockIdx.y * 32;
  f32x16 ag = {}, au = {};
  for (int k = 0; k < DM; k += 16) {
    bf16x8 xa = *(const bf16x8*)(x + (size_t)(m0 + r) * DM + k + 8 * hh);
    bf16x8 g, u;
    for (int j = 0; j < 8; ++j) { g[j] = wg[(size_t)(k + 8 * hh + j) * DF + f0 + r]; u[j] = wu[(size_t)(k + 8 * hh + j) * DF + f0 + r]; }
    ag = __builtin_amdgcn_mfma_f32_32x32x16_bf16(xa, g, ag, 0, 0, 0);
    au = __builtin_amdgcn_mfma_f32_32x32x16_bf16(xa, u, au, 0, 0, 0);
  }
  for (int i = 0; i < 16; ++i) {
    int row = (i & 3) + 8 * (i >> 2) + 4 * hh;
    float g = rbf(ag[i]), u = rbf(au[i]);
    float y = 0.7978845608028654f * (g + 0.044715f * g * g * g);
    float ge = rbf(0.5f * g * (1.f + tanhf(y)));
    h[(size_t)(m0 + row) * DF + f0 + r] = f2bf(ge * u);
  }
}
__global__ __launch_bounds__(64) void k2(const u16* h, const u16* wd, u16* out) {
  int lane = threadIdx.x, r = lane & 31, hh = lane >> 5;
  int n0 = blockIdx.x * 32, m0 = blockIdx.y * 32;
  f32x16 a = {};
  for (int k = 0; k < DF; k += 16) {
    bf16x8 xa = *(const bf16x8*)(h + (size_t)(m0 + r) * DF + k + 8 * hh);
    bf16x8 g;
    for (int j = 0; j < 8; ++j) g[j] = wd[(size_t)(k + 8 * hh + j) * DM + n0 + r];
    a = __builtin_amdgcn_mfma_f32_32x32x16_bf16(xa, g, a, 0, 0, 0);
  }
  for (int i = 0; i < 16; ++i) {
    int row = (i & 3) + 8 * (i >> 2) + 4 * hh;
    out[(size_t)(m0 + row) * DM + n0 + r] = f2bf(a[i]);
  }
}

extern "C" void kernel_launch(void* const* d_in, const int* in_sizes, int n_in,
                              void* d_out, int out_size, void* d_ws, size_t ws_size,
                              hipStream_t stream) {
  const u16* x = (const u16*)d_in[0];
  const u16* wg = (const u16*)d_in[1];
  const u16* wu = (const u16*)d_in[2];
  const u16* wd = (const u16*)d_in[3];
  u16* h = (u16*)d_ws;
  k1<<<dim3(DF / 32, S / 32), 64, 0, stream>>>(x, wg, wu, h);
  k2<<<dim3(DM / 32, S / 32), 64, 0, stream>>>(h, wd, (u16*)d_out);
}
```

```cpp
#include <hip/hip_runtime.h>
#include <hip/hip_bf16.h>

typedef short s16x4 __attribute__((ext_vector_type(4)));
typedef short s16x8 __attribute__((ext_vector_type(8)));
typedef float f32x2 __attribute__((ext_vector_type(2)));
typedef float f32x4 __attribute__((ext_vector_type(4)));
typedef float f32x16 __attribute__((ext_vector_type(16)));
typedef unsigned u32x4 __attribute__((ext_vector_type(4)));
typedef __bf16 bf16x2 __attribute__((ext_vector_type(2)));
typedef unsigned long long u64;
#define LAS __attribute__((address_space(3)))
#define GAS __attribute__((address_space(1)))
#define DI __device__ __forceinline__

constexpr int S = 2048, DM = 512, DF = 1376;
constexpr int NCH = 43;
constexpr int WPITCH = DF * 2;
constexpr int STAGE = 40960;
constexpr int LDS_BYTES = 163840;
constexpr int WS_FLAG = 4096;
constexpr int WS_H = 65536;
constexpr int H_BYTES = 64 * NCH * 2048;
constexpr unsigned SPIN_CAP = 1u << 16;

#define STAMP(i) do {} while (0)

#define BARRIER() asm volatile("s_waitcnt lgkmcnt(0)\n\ts_barrier" ::: "memory")
#define WAITV(n) asm volatile("s_waitcnt vmcnt(" #n ")" ::: "memory")

DI void dma4(unsigned v0, unsigned v1, unsigned v2, unsigned v3, const char* sbase, unsigned lds) {
  asm volatile("s_mov_b32 m0, %5\n\ts_nop 0\n\t"
               "global_load_lds_dwordx4 %0, %4\n\t"
               "global_load_lds_dwordx4 %1, %4 offset:1024\n\t"
               "global_load_lds_dwordx4 %2, %4 offset:2048\n\t"
               "global_load_lds_dwordx4 %3, %4 offset:3072"
               ::"v"(v0), "v"(v1), "v"(v2), "v"(v3), "s"(sbase), "s"(lds) : "memory");
}
DI void dma3(unsigned v0, unsigned v1, unsigned v2, const char* sbase, unsigned lds) {
  asm volatile("s_mov_b32 m0, %4\n\ts_nop 0\n\t"
               "global_load_lds_dwordx4 %0, %3\n\t"
               "global_load_lds_dwordx4 %1, %3 offset:1024\n\t"
               "global_load_lds_dwordx4 %2, %3 offset:2048"
               ::"v"(v0), "v"(v1), "v"(v2), "s"(sbase), "s"(lds) : "memory");
}
DI s16x8 trfrag(unsigned a0, unsigned a1) {
  s16x4 lo = __builtin_amdgcn_ds_read_tr16_b64_v4i16((LAS s16x4*)a0);
  s16x4 hi = __builtin_amdgcn_ds_read_tr16_b64_v4i16((LAS s16x4*)a1);
  return __builtin_shufflevector(lo, hi, 0, 1, 2, 3, 4, 5, 6, 7);
}
DI unsigned pack2(float a, float b) {
  f32x2 v = {a, b};
  return __builtin_bit_cast(unsigned, __builtin_convertvector(v, bf16x2));
}
DI float geglu(float g, float u) {
  constexpr float K0 = -2.0f * 0.7978845608028654f * 1.4426950408889634f;
  constexpr float K1 = K0 * 0.044715f;
  float z = g * __builtin_fmaf(g * g, K1, K0);
  float e = __builtin_amdgcn_exp2f(z);
  return g * __builtin_amdgcn_rcpf(1.0f + e) * u;
}
#define MFMA(a, b, c) __builtin_amdgcn_mfma_f32_32x32x16_bf16((a), (b), (c), 0, 0, 0)

struct Frag1 { s16x8 x, g[3], u[3]; };

__global__ __launch_bounds__(384, 2) void geglu_kernel(const char* __restrict__ x, const char* __restrict__ wg,
                                                       const char* __restrict__ wu, const char* __restrict__ wd,
                                                       char* __restrict__ out, char* __restrict__ ws
) {
  __shared__ __attribute__((aligned(16))) char smem[LDS_BYTES];
  const unsigned lds0 = (unsigned)(size_t)(LAS char*)smem;
  const int lane = threadIdx.x & 63;
  const int w = __builtin_amdgcn_readfirstlane(threadIdx.x >> 6);
  const int b = blockIdx.x, xcd = b & 7, bi = b >> 3, a = xcd & 3;
  const int p = (xcd >> 2) * 8 + (bi >> 2), j = a * 4 + (bi & 3);
  const int rh = a & 1, nt = (a >> 1) * 4 + (bi & 3);
  const int c0 = j < 14 ? 3 * j : 40;
  const int rot = (p + j) & 7;
  const bool prod = j < 15;
  STAMP(0);

  if (w >= 4) {
    const int rip = lane >> 3, pc = lane & 7;
    const unsigned dsrc = rip * 1024 + ((pc ^ (((rip >> 1) & 1) << 2)) << 4);
    const unsigned d0 = dsrc, d1 = dsrc + 7168, d2 = dsrc + 2 * 7168, d3 = dsrc + 3 * 7168;
    const char* wdb = wd + nt * 128;
    const int lw = w - 4;
    auto issue_wd = [&](int ps) {
#pragma unroll
      for (int g = 0; g < 5; ++g) { const int G = ps * 10 + 2 * g + lw; dma4(d0, d1, d2, d3, wdb + (size_t)G * 32768, lds0 + G * 4096); }
    };
    if (prod) {
      if (lw == 0) {
        const unsigned ve = rip * 1024 + ((pc ^ (rip >> 1)) << 4), vo = ve ^ 64;
        const unsigned v0 = ve, v1 = vo + 7168, v2 = ve + 2 * 7168, v3 = vo + 3 * 7168;
        const char* xb = x + (size_t)p * 131072;
        auto issue = [&](int s) {
          const int kc = (s + rot) & 7;
#pragma unroll
          for (int g = 0; g < 4; ++g) dma4(v0, v1, v2, v3, xb + g * 32768 + kc * 128, lds0 + (s & 3) * STAGE + g * 4096);
        };
        issue(0); issue(1);
        for (int s = 0; s < 6; ++s) { WAITV(16); BARRIER(); issue(s + 2); }
        WAITV(16); BARRIER(); issue_wd(0);
        WAITV(20); BARRIER(); issue_wd(1); issue_wd(2);
      } else {
        unsigned v[3];
#pragma unroll
        for (int i = 0; i < 3; ++i) { const int u = i * 64 + lane, row = u / 12, col = u - row * 12; v[i] = row * WPITCH + col * 16 - i * 1024; }
        const char* gb = wg + c0 * 64;
        const char* ub = wu + c0 * 64;
        auto issue = [&](int s) {
          const int kc = (s + rot) & 7;
#pragma unroll
          for (int g = 0; g < 4; ++g) {
            const size_t ro = (size_t)(kc * 64 + g * 16) * WPITCH;
            dma3(v[0], v[1], v[2], gb + ro, lds0 + (s & 3) * STAGE + 16384 + g * 3072);
            dma3(v[0], v[1], v[2], ub + ro, lds0 + (s & 3) * STAGE + 16384 + 12288 + g * 3072);
          }
        };
        issue(0); issue(1);
        for (int s = 0; s < 6; ++s) { WAITV(24); BARRIER(); issue(s + 2); }
        WAITV(24); BARRIER(); issue_wd(0);
        WAITV(20); BARRIER(); issue_wd(1); issue_wd(2);
      }
      STAMP(1);
      BARRIER();
      issue_wd(3);
    } else {
      issue_wd(0); issue_wd(1); issue_wd(2); issue_wd(3);
    }
    WAITV(0);
    STAMP(2);
    BARRIER();
    BARRIER();
    if (lw == 0) { dma4(d0, d1, d2, d3, wdb + (size_t)40 * 32768, lds0); dma4(d0, d1, d2, d3, wdb + (size_t)42 * 32768, lds0 + 8192); }
    else dma4(d0, d1, d2, d3, wdb + (size_t)41 * 32768, lds0 + 4096);
    WAITV(0);
    BARRIER();
    if (j == 15 && lw == 0) {
      GAS unsigned* ep = (GAS unsigned*)(ws + p * 256);
      const unsigned e0 = __hip_atomic_load(ep, __ATOMIC_RELAXED, __HIP_MEMORY_SCOPE_AGENT);
      const unsigned tag = e0 * 1664525u + 1013904223u;
      GAS unsigned* fl = (GAS unsigned*)(ws + WS_FLAG + p * 256) + lane;
      for (unsigned spin = 0; spin < SPIN_CAP; ++spin) {
        if (__all(__hip_atomic_load(fl, __ATOMIC_RELAXED, __HIP_MEMORY_SCOPE_AGENT) == tag)) break;
        __builtin_amdgcn_s_sleep(2);
      }
      if (lane == 0) __hip_atomic_store(ep, tag, __ATOMIC_RELAXED, __HIP_MEMORY_SCOPE_AGENT);
    }
    return;
  }

  const unsigned e0 = __hip_atomic_load((GAS unsigned*)(ws + p * 256), __ATOMIC_RELAXED, __HIP_MEMORY_SCOPE_AGENT);
  const int r = lane & 31, hh = lane >> 5, blk = (lane >> 4) & 1, q = (lane & 15) >> 2, pp = lane & 3;
  const __amdgpu_buffer_rsrc_t hrs = __builtin_amdgcn_make_buffer_rsrc(ws + WS_H, 0, H_BYTES, 0x00020000);

  if (prod) {
    f32x16 ag[3], au[3];
#pragma unroll
    for (int c = 0; c < 3; ++c) { ag[c] = (f32x16)(0.f); au[c] = (f32x16)(0.f); }
    const int xrow = w * 32 + r;
    const unsigned xa0 = lds0 + xrow * 128 + ((hh ^ ((xrow >> 1) & 7)) << 4);
    const unsigned wa0 = lds0 + 16384 + (8 * hh + q) * 192 + blk * 32 + pp * 8;
    auto load = [&](Frag1& f, unsigned sb, int ks) {
      f.x = *(LAS s16x8*)((xa0 ^ (ks * 32)) + sb);
      const unsigned wa = wa0 + sb + ks * 3072;
#pragma unroll
      for (int c = 0; c < 3; ++c) {
        f.g[c] = trfrag(wa + c * 64, wa + c * 64 + 768);
        f.u[c] = trfrag(wa + 12288 + c * 64, wa + 12288 + c * 64 + 768);
      }
    };
    auto mma = [&](const Frag1& f) {
#pragma unroll
      for (int c = 0; c < 3; ++c) { ag[c] = MFMA(f.g[c], f.x, ag[c]); au[c] = MFMA(f.u[c], f.x, au[c]); }
    };
    Frag1 f0, f1;
    BARRIER();
    STAMP(1);
    load(f0, 0, 0);
    for (int s = 0; s < 8; ++s) {
      const unsigned sb = (s & 3) * STAGE;
      load(f1, sb, 1); mma(f0);
      load(f0, sb, 2); mma(f1);
      load(f1, sb, 3); mma(f0);
      BARRIER();
      if (s < 7) load(f0, ((s + 1) & 3) * STAGE, 0);
      mma(f1);
    }
    STAMP(2);
    const int hbase = ((p * 4 + w) * NCH + c0) * 2048 + lane * 16;
#pragma unroll
    for (int c = 0; c < 3; ++c) {
      if (j < 14 || c == 2) {
#pragma unroll
        for (int s = 0; s < 2; ++s) {
          u32x4 v;
#pragma unroll
          for (int i = 0; i < 4; ++i)
            v[i] = pack2(geglu(ag[c][8 * s + 2 * i], au[c][8 * s + 2 * i]), geglu(ag[c][8 * s + 2 * i + 1], au[c][8 * s + 2 * i + 1]));
          __builtin_amdgcn_raw_buffer_store_b128(v, hrs, hbase + c * 2048 + s * 1024, 0, 16);
        }
      }
    }
    STAMP(3);
    WAITV(0);
    STAMP(4);
  }
  const unsigned tag = e0 * 1664525u + 1013904223u;
  GAS unsigned* flags = (GAS unsigned*)(ws + WS_FLAG + p * 256);
  if (lane == 0) __hip_atomic_store(flags + w * 16 + j, tag, __ATOMIC_RELAXED, __HIP_MEMORY_SCOPE_AGENT);

  const int mh = w & 1, kh = w >> 1, mb = 2 * rh + mh;
  BARRIER();
  STAMP(5);
  for (unsigned spin = 0; spin < SPIN_CAP; ++spin) {
    if (__all(__hip_atomic_load(flags + mb * 16 + (lane & 15), __ATOMIC_RELAXED, __HIP_MEMORY_SCOPE_AGENT) == tag)) break;
    __builtin_amdgcn_s_sleep(1);
  }
  __builtin_amdgcn_fence(__ATOMIC_ACQUIRE, "wavefront");
  STAMP(6);
  u32x4 hf[22][2];
  {
    const int hb = ((p * 4 + mb) * NCH + kh) * 2048 + lane * 16;
#pragma unroll
    for (int i = 0; i < 22; ++i) {
      const int off = hb + (i < 21 ? i * 4096 : (kh ? 20 * 4096 : 21 * 4096));
      hf[i][0] = __builtin_amdgcn_raw_buffer_load_b128(hrs, off, 0, 16);
      hf[i][1] = __builtin_amdgcn_raw_buffer_load_b128(hrs, off + 1024, 0, 16);
    }
    __builtin_amdgcn_sched_barrier(0);
  }
  f32x16 o0 = (f32x16)(0.f), o1 = (f32x16)(0.f);
  const unsigned da0 = lds0 + kh * 4096 + (4 * hh + q) * 128 + ((q >> 1) << 6) + (16 * (pp & 1) + 8 * blk + 4 * (pp >> 1)) * 2;
  const unsigned da1 = da0 ^ 64;
#pragma unroll
  for (int i = 0; i < 22; ++i) {
    if (i == 2) BARRIER();
    if (i == 20) BARRIER();
    if (i < 21 || kh == 0) {
      const unsigned off = (i % 20) * 8192;
#pragma unroll
      for (int s = 0; s < 2; ++s) {
        const s16x8 a0 = trfrag(da0 + off + s * 2048, da0 + off + s * 2048 + 1024);
        const s16x8 a1 = trfrag(da1 + off + s * 2048, da1 + off + s * 2048 + 1024);
        const s16x8 hb = __builtin_bit_cast(s16x8, hf[i][s]);
        o0 = MFMA(a0, hb, o0);
        o1 = MFMA(a1, hb, o1);
      }
    }
  }
  STAMP(7);
  {
    LAS f32x4* xw = (LAS f32x4*)(size_t)(lds0 + 16384 + w * 4096 + lane * 16);
    const f32x16 snd = kh ? o0 : o1;
#pragma unroll
    for (int g = 0; g < 4; ++g) xw[g * 64] = (f32x4){snd[4 * g], snd[4 * g + 1], snd[4 * g + 2], snd[4 * g + 3]};
    BARRIER();
    LAS f32x4* xr = (LAS f32x4*)(size_t)(lds0 + 16384 + (w ^ 2) * 4096 + lane * 16);
    f32x16 kp = kh ? o1 : o0;
    u32x4 v[2];
#pragma unroll
    for (int g = 0; g < 4; ++g) {
      const f32x4 t = xr[g * 64];
      v[g >> 1][(g & 1) * 2] = pack2(kp[4 * g] + t[0], kp[4 * g + 1] + t[1]);
      v[g >> 1][(g & 1) * 2 + 1] = pack2(kp[4 * g + 2] + t[2], kp[4 * g + 3] + t[3]);
    }
    char* op = out + (size_t)(p * 128 + mb * 32 + r) * 1024 + nt * 128 + kh * 64 + hh * 32;
    *(GAS u32x4*)op = v[0];
    *(GAS u32x4*)(op + 16) = v[1];
  }
  STAMP(8);
}

extern "C" void kernel_launch(void* const* d_in, const int* in_sizes, int n_in, void* d_out, int out_size, void* d_ws,
                              size_t ws_size, hipStream_t stream) {
  geglu_kernel<<<256, 384, 0, stream>>>((const char*)d_in[0], (const char*)d_in[1], (const char*)d_in[2],
                                        (const char*)d_in[3], (char*)d_out, (char*)d_ws);
}
```

```cpp
#include <hip/hip_runtime.h>
#include <hip/hip_bf16.h>

typedef short s16x4 __attribute__((ext_vector_type(4)));
typedef short s16x8 __attribute__((ext_vector_type(8)));
typedef float f32x2 __attribute__((ext_vector_type(2)));
typedef float f32x4 __attribute__((ext_vector_type(4)));
typedef float f32x16 __attribute__((ext_vector_type(16)));
typedef unsigned u32x4 __attribute__((ext_vector_type(4)));
typedef __bf16 bf16x2 __attribute__((ext_vector_type(2)));
typedef unsigned long long u64;
#define LAS __attribute__((address_space(3)))
#define GAS __attribute__((address_space(1)))
#define DI __device__ __forceinline__

constexpr int S = 2048, DM = 512, DF = 1376;
constexpr int NCH = 43;
constexpr int WPITCH = DF * 2;
constexpr int STAGE = 40960;
constexpr int LDS_BYTES = 163840;
constexpr int WS_FLAG = 4096;
constexpr int WS_H = 65536;
constexpr int H_BYTES = 64 * NCH * 2048;
constexpr unsigned SPIN_CAP = 1u << 16;

#define STAMP(i) do {} while (0)

#define BARRIER() asm volatile("s_waitcnt lgkmcnt(0)\n\ts_barrier" ::: "memory")
#define WAITV(n) asm volatile("s_waitcnt vmcnt(" #n ")" ::: "memory")

DI void dma4(unsigned v0, unsigned v1, unsigned v2, unsigned v3, const char* sbase, unsigned lds) {
  asm volatile("s_mov_b32 m0, %5\n\ts_nop 0\n\t"
               "global_load_lds_dwordx4 %0, %4\n\t"
               "global_load_lds_dwordx4 %1, %4 offset:1024\n\t"
               "global_load_lds_dwordx4 %2, %4 offset:2048\n\t"
               "global_load_lds_dwordx4 %3, %4 offset:3072"
               ::"v"(v0), "v"(v1), "v"(v2), "v"(v3), "s"(sbase), "s"(lds) : "memory");
}
DI void dma3(unsigned v0, unsigned v1, unsigned v2, const char* sbase, unsigned lds) {
  asm volatile("s_mov_b32 m0, %4\n\ts_nop 0\n\t"
               "global_load_lds_dwordx4 %0, %3\n\t"
               "global_load_lds_dwordx4 %1, %3 offset:1024\n\t"
               "global_load_lds_dwordx4 %2, %3 offset:2048"
               ::"v"(v0), "v"(v1), "v"(v2), "s"(sbase), "s"(lds) : "memory");
}
DI s16x8 trfrag(unsigned a0, unsigned a1) {
  s16x4 lo = __builtin_amdgcn_ds_read_tr16_b64_v4i16((LAS s16x4*)a0);
  s16x4 hi = __builtin_amdgcn_ds_read_tr16_b64_v4i16((LAS s16x4*)a1);
  return __builtin_shufflevector(lo, hi, 0, 1, 2, 3, 4, 5, 6, 7);
}
DI unsigned pack2(float a, float b) {
  f32x2 v = {a, b};
  return __builtin_bit_cast(unsigned, __builtin_convertvector(v, bf16x2));
}
DI float geglu(float g, float u) {
  constexpr float K0 = -2.0f * 0.7978845608028654f * 1.4426950408889634f;
  constexpr float K1 = K0 * 0.044715f;
  float z = g * __builtin_fmaf(g * g, K1, K0);
  float e = __builtin_amdgcn_exp2f(z);
  return g * __builtin_amdgcn_rcpf(1.0f + e) * u;
}
#define MFMA(a, b, c) __builtin_amdgcn_mfma_f32_32x32x16_bf16((a), (b), (c), 0, 0, 0)

struct Frag1 { s16x8 x, g[3], u[3]; };

__global__ __launch_bounds__(384, 2) void geglu_kernel(const char* __restrict__ x, const char* __restrict__ wg,
                                                       const char* __restrict__ wu, const char* __restrict__ wd,
                                                       char* __restrict__ out, char* __restrict__ ws
) {
  __shared__ __attribute__((aligned(16))) char smem[LDS_BYTES];
  const unsigned lds0 = (unsigned)(size_t)(LAS char*)smem;
  const int lane = threadIdx.x & 63;
  const int w = __builtin_amdgcn_readfirstlane(threadIdx.x >> 6);
  const int b = blockIdx.x, xcd = b & 7, bi = b >> 3, a = xcd & 3;
  const int p = (xcd >> 2) * 8 + (bi >> 2), j = a * 4 + (bi & 3);
  const int rh = a & 1, nt = (a >> 1) * 4 + (bi & 3);
  const int c0 = j < 14 ? 3 * j : 40;
  const int rot = (p + j) & 7;
  const bool prod = j < 15;
  { u64 c0_ = __builtin_amdgcn_s_memrealtime();
    unsigned acc_ = 0; const int tid_ = threadIdx.x, bi_ = blockIdx.x >> 3;
    for (int i = bi_ * 384 + tid_; i < 16384; i += 32 * 384) acc_ += *(const GAS unsigned*)(x + (size_t)i * 128);
    for (int i = bi_ * 384 + tid_; i < 11008; i += 32 * 384) acc_ += *(const GAS unsigned*)(wg + (size_t)i * 128) + *(const GAS unsigned*)(wu + (size_t)i * 128) + *(const GAS unsigned*)(wd + (size_t)i * 128);
    { u64 pc_; asm volatile("s_getpc_b64 %0" : "=s"(pc_)); pc_ &= ~255ull;
      if (tid_ < 192) acc_ += *(const GAS unsigned*)(pc_ + tid_ * 128); }

    asm volatile("s_waitcnt vmcnt(0)" :: "v"(acc_) : "memory");
    while (__builtin_amdgcn_s_memrealtime() - c0_ < 1000) {} }

  if (w >= 4) {
    const int rip = lane >> 3, pc = lane & 7;
    const unsigned dsrc = rip * 1024 + ((pc ^ (((rip >> 1) & 1) << 2)) << 4);
    const unsigned d0 = dsrc, d1 = dsrc + 7168, d2 = dsrc + 2 * 7168, d3 = dsrc + 3 * 7168;
    const char* wdb = wd + nt * 128;
    const int lw = w - 4;
    auto issue_wd = [&](int ps) {
#pragma unroll
      for (int g = 0; g < 5; ++g) { const int G = ps * 10 + 2 * g + lw; dma4(d0, d1, d2, d3, wdb + (size_t)G * 32768, lds0 + G * 4096); }
    };
    if (prod) {
      if (lw == 0) {
        const unsigned ve = rip * 1024 + ((pc ^ (rip >> 1)) << 4), vo = ve ^ 64;
        const unsigned v0 = ve, v1 = vo + 7168, v2 = ve + 2 * 7168, v3 = vo + 3 * 7168;
        const char* xb = x + (size_t)p * 131072;
        auto issue = [&](int s) {
          const int kc = (s + rot) & 7;
#pragma unroll
          for (int g = 0; g < 4; ++g) dma4(v0, v1, v2, v3, xb + g * 32768 + kc * 128, lds0 + (s & 3) * STAGE + g * 4096);
        };
        issue(0); issue(1);
        for (int s = 0; s < 6; ++s) { WAITV(16); BARRIER(); issue(s + 2); }
        WAITV(16); BARRIER(); issue_wd(0);
        WAITV(20); BARRIER(); issue_wd(1); issue_wd(2);
      } else {
        unsigned v[3];
#pragma unroll
        for (int i = 0; i < 3; ++i) { const int u = i * 64 + lane, row = u / 12, col = u - row * 12; v[i] = row * WPITCH + col * 16 - i * 1024; }
        const char* gb = wg + c0 * 64;
        const char* ub = wu + c0 * 64;
        auto issue = [&](int s) {
          const int kc = (s + rot) & 7;
#pragma unroll
          for (int g = 0; g < 4; ++g) {
            const size_t ro = (size_t)(kc * 64 + g * 16) * WPITCH;
            dma3(v[0], v[1], v[2], gb + ro, lds0 + (s & 3) * STAGE + 16384 + g * 3072);
            dma3(v[0], v[1], v[2], ub + ro, lds0 + (s & 3) * STAGE + 16384 + 12288 + g * 3072);
          }
        };
        issue(0); issue(1);
        for (int s = 0; s < 6; ++s) { WAITV(24); BARRIER(); issue(s + 2); }
        WAITV(24); BARRIER(); issue_wd(0);
        WAITV(20); BARRIER(); issue_wd(1); issue_wd(2);
      }
      STAMP(1);
      BARRIER();
      issue_wd(3);
    } else {
      issue_wd(0); issue_wd(1); issue_wd(2); issue_wd(3);
    }
    WAITV(0);
    STAMP(2);
    BARRIER();
    BARRIER();
    if (lw == 0) { dma4(d0, d1, d2, d3, wdb + (size_t)40 * 32768, lds0); dma4(d0, d1, d2, d3, wdb + (size_t)42 * 32768, lds0 + 8192); }
    else dma4(d0, d1, d2, d3, wdb + (size_t)41 * 32768, lds0 + 4096);
    WAITV(0);
    BARRIER();
    if (j == 15 && lw == 0) {
      GAS unsigned* ep = (GAS unsigned*)(ws + p * 256);
      const unsigned e0 = __hip_atomic_load(ep, __ATOMIC_RELAXED, __HIP_MEMORY_SCOPE_AGENT);
      const unsigned tag = e0 * 1664525u + 1013904223u;
      GAS unsigned* fl = (GAS unsigned*)(ws + WS_FLAG + p * 256) + lane;
      for (unsigned spin = 0; spin < SPIN_CAP; ++spin) {
        if (__all(__hip_atomic_load(fl, __ATOMIC_RELAXED, __HIP_MEMORY_SCOPE_AGENT) == tag)) break;
        __builtin_amdgcn_s_sleep(2);
      }
      if (lane == 0) __hip_atomic_store(ep, tag, __ATOMIC_RELAXED, __HIP_MEMORY_SCOPE_AGENT);
    }
    return;
  }

  const unsigned e0 = __hip_atomic_load((GAS unsigned*)(ws + p * 256), __ATOMIC_RELAXED, __HIP_MEMORY_SCOPE_AGENT);
  const int r = lane & 31, hh = lane >> 5, blk = (lane >> 4) & 1, q = (lane & 15) >> 2, pp = lane & 3;
  const __amdgpu_buffer_rsrc_t hrs = __builtin_amdgcn_make_buffer_rsrc(ws + WS_H, 0, H_BYTES, 0x00020000);

  if (prod) {
    f32x16 ag[3], au[3];
#pragma unroll
    for (int c = 0; c < 3; ++c) { ag[c] = (f32x16)(0.f); au[c] = (f32x16)(0.f); }
    const int xrow = w * 32 + r;
    const unsigned xa0 = lds0 + xrow * 128 + ((hh ^ ((xrow >> 1) & 7)) << 4);
    const unsigned wa0 = lds0 + 16384 + (8 * hh + q) * 192 + blk * 32 + pp * 8;
    auto load = [&](Frag1& f, unsigned sb, int ks) {
      f.x = *(LAS s16x8*)((xa0 ^ (ks * 32)) + sb);
      const unsigned wa = wa0 + sb + ks * 3072;
#pragma unroll
      for (int c = 0; c < 3; ++c) {
        f.g[c] = trfrag(wa + c * 64, wa + c * 64 + 768);
        f.u[c] = trfrag(wa + 12288 + c * 64, wa + 12288 + c * 64 + 768);
      }
    };
    auto mma = [&](const Frag1& f) {
#pragma unroll
      for (int c = 0; c < 3; ++c) { ag[c] = MFMA(f.g[c], f.x, ag[c]); au[c] = MFMA(f.u[c], f.x, au[c]); }
    };
    Frag1 f0, f1;
    BARRIER();
    STAMP(1);
    load(f0, 0, 0);
    for (int s = 0; s < 8; ++s) {
      const unsigned sb = (s & 3) * STAGE;
      load(f1, sb, 1); mma(f0);
      load(f0, sb, 2); mma(f1);
      load(f1, sb, 3); mma(f0);
      BARRIER();
      if (s < 7) load(f0, ((s + 1) & 3) * STAGE, 0);
      mma(f1);
    }
    STAMP(2);
    const int hbase = ((p * 4 + w) * NCH + c0) * 2048 + lane * 16;
#pragma unroll
    for (int c = 0; c < 3; ++c) {
      if (j < 14 || c == 2) {
#pragma unroll
        for (int s = 0; s < 2; ++s) {
          u32x4 v;
#pragma unroll
          for (int i = 0; i < 4; ++i)
            v[i] = pack2(geglu(ag[c][8 * s + 2 * i], au[c][8 * s + 2 * i]), geglu(ag[c][8 * s + 2 * i + 1], au[c][8 * s + 2 * i + 1]));
          __builtin_amdgcn_raw_buffer_store_b128(v, hrs, hbase + c * 2048 + s * 1024, 0, 16);
        }
      }
    }
    STAMP(3);
    WAITV(0);
    STAMP(4);
  }
  const unsigned tag = e0 * 1664525u + 1013904223u;
  GAS unsigned* flags = (GAS unsigned*)(ws + WS_FLAG + p * 256);
  if (lane == 0) __hip_atomic_store(flags + w * 16 + j, tag, __ATOMIC_RELAXED, __HIP_MEMORY_SCOPE_AGENT);

  const int mh = w & 1, kh = w >> 1, mb = 2 * rh + mh;
  BARRIER();
  STAMP(5);
  for (unsigned spin = 0; spin < SPIN_CAP; ++spin) {
    if (__all(__hip_atomic_load(flags + mb * 16 + (lane & 15), __ATOMIC_RELAXED, __HIP_MEMORY_SCOPE_AGENT) == tag)) break;
    __builtin_amdgcn_s_sleep(1);
  }
  __builtin_amdgcn_fence(__ATOMIC_ACQUIRE, "wavefront");
  STAMP(6);
  u32x4 hf[22][2];
  {
    const int hb = ((p * 4 + mb) * NCH + kh) * 2048 + lane * 16;
#pragma unroll
    for (int i = 0; i < 22; ++i) {
      const int off = hb + (i < 21 ? i * 4096 : (kh ? 20 * 4096 : 21 * 4096));
      hf[i][0] = __builtin_amdgcn_raw_buffer_load_b128(hrs, off, 0, 16);
      hf[i][1] = __builtin_amdgcn_raw_buffer_load_b128(hrs, off + 1024, 0, 16);
    }
    __builtin_amdgcn_sched_barrier(0);
  }
  f32x16 o0 = (f32x16)(0.f), o1 = (f32x16)(0.f);
  const unsigned da0 = lds0 + kh * 4096 + (4 * hh + q) * 128 + ((q >> 1) << 6) + (16 * (pp & 1) + 8 * blk + 4 * (pp >> 1)) * 2;
  const unsigned da1 = da0 ^ 64;
#pragma unroll
  for (int i = 0; i < 22; ++i) {
    if (i == 2) BARRIER();
    if (i == 20) BARRIER();
    if (i < 21 || kh == 0) {
      const unsigned off = (i % 20) * 8192;
#pragma unroll
      for (int s = 0; s < 2; ++s) {
        const s16x8 a0 = trfrag(da0 + off + s * 2048, da0 + off + s * 2048 + 1024);
        const s16x8 a1 = trfrag(da1 + off + s * 2048, da1 + off + s * 2048 + 1024);
        const s16x8 hb = __builtin_bit_cast(s16x8, hf[i][s]);
        o0 = MFMA(a0, hb, o0);
        o1 = MFMA(a1, hb, o1);
      }
    }
  }
  STAMP(7);
  {
    LAS f32x4* xw = (LAS f32x4*)(size_t)(lds0 + 16384 + w * 4096 + lane * 16);
    const f32x16 snd = kh ? o0 : o1;
#pragma unroll
    for (int g = 0; g < 4; ++g) xw[g * 64] = (f32x4){snd[4 * g], snd[4 * g + 1], snd[4 * g + 2], snd[4 * g + 3]};
    BARRIER();
    LAS f32x4* xr = (LAS f32x4*)(size_t)(lds0 + 16384 + (w ^ 2) * 4096 + lane * 16);
    f32x16 kp = kh ? o1 : o0;
    u32x4 v[2];
#pragma unroll
    for (int g = 0; g < 4; ++g) {
      const f32x4 t = xr[g * 64];
      v[g >> 1][(g & 1) * 2] = pack2(kp[4 * g] + t[0], kp[4 * g + 1] + t[1]);
      v[g >> 1][(g & 1) * 2 + 1] = pack2(kp[4 * g + 2] + t[2], kp[4 * g + 3] + t[3]);
    }
    char* op = out + (size_t)(p * 128 + mb * 32 + r) * 1024 + nt * 128 + kh * 64 + hh * 32;
    *(GAS u32x4*)op = v[0];
    *(GAS u32x4*)(op + 16) = v[1];
  }
  STAMP(8);
}

extern "C" void kernel_launch(void* const* d_in, const int* in_sizes, int n_in, void* d_out, int out_size, void* d_ws,
                              size_t ws_size, hipStream_t stream) {
  geglu_kernel<<<256, 384, 0, stream>>>((const char*)d_in[0], (const char*)d_in[1], (const char*)d_in[2],
                                        (const char*)d_in[3], (char*)d_out, (char*)d_ws);
}
```

```cpp
#include <hip/hip_runtime.h>
#include <hip/hip_bf16.h>

typedef short s16x4 __attribute__((ext_vector_type(4)));
typedef short s16x8 __attribute__((ext_vector_type(8)));
typedef float f32x2 __attribute__((ext_vector_type(2)));
typedef float f32x4 __attribute__((ext_vector_type(4)));
typedef float f32x16 __attribute__((ext_vector_type(16)));
typedef unsigned u32x4 __attribute__((ext_vector_type(4)));
typedef __bf16 bf16x2 __attribute__((ext_vector_type(2)));
typedef unsigned long long u64;
#define LAS __attribute__((address_space(3)))
#define GAS __attribute__((address_space(1)))
#define DI __device__ __forceinline__

constexpr int S = 2048, DM = 512, DF = 1376;
constexpr int NCH = 43;
constexpr int WPITCH = DF * 2;
constexpr int STAGE = 40960;
constexpr int LDS_BYTES = 163840;
constexpr int WS_FLAG = 4096;
constexpr int WS_H = 65536;
constexpr int H_BYTES = 64 * NCH * 2048;
constexpr unsigned SPIN_CAP = 1u << 16;

#define STAMP(i) do {} while (0)

#define BARRIER() asm volatile("s_waitcnt lgkmcnt(0)\n\ts_barrier" ::: "memory")
#define WAITV(n) asm volatile("s_waitcnt vmcnt(" #n ")" ::: "memory")

DI void dma4(unsigned v0, unsigned v1, unsigned v2, unsigned v3, const char* sbase, unsigned lds) {
  asm volatile("s_mov_b32 m0, %5\n\ts_nop 0\n\t"
               "global_load_lds_dwordx4 %0, %4\n\t"
               "global_load_lds_dwordx4 %1, %4 offset:1024\n\t"
               "global_load_lds_dwordx4 %2, %4 offset:2048\n\t"
               "global_load_lds_dwordx4 %3, %4 offset:3072"
               ::"v"(v0), "v"(v1), "v"(v2), "v"(v3), "s"(sbase), "s"(lds) : "memory");
}
DI void dma3(unsigned v0, unsigned v1, unsigned v2, const char* sbase, unsigned lds) {
  asm volatile("s_mov_b32 m0, %4\n\ts_nop 0\n\t"
               "global_load_lds_dwordx4 %0, %3\n\t"
               "global_load_lds_dwordx4 %1, %3 offset:1024\n\t"
               "global_load_lds_dwordx4 %2, %3 offset:2048"
               ::"v"(v0), "v"(v1), "v"(v2), "s"(sbase), "s"(lds) : "memory");
}
DI s16x8 trfrag(unsigned a0, unsigned a1) {
  s16x4 lo = __builtin_amdgcn_ds_read_tr16_b64_v4i16((LAS s16x4*)a0);
  s16x4 hi = __builtin_amdgcn_ds_read_tr16_b64_v4i16((LAS s16x4*)a1);
  return __builtin_shufflevector(lo, hi, 0, 1, 2, 3, 4, 5, 6, 7);
}
DI unsigned pack2(float a, float b) {
  f32x2 v = {a, b};
  return __builtin_bit_cast(unsigned, __builtin_convertvector(v, bf16x2));
}
DI float geglu(float g, float u) {
  constexpr float K0 = -2.0f * 0.7978845608028654f * 1.4426950408889634f;
  constexpr float K1 = K0 * 0.044715f;
  float z = g * __builtin_fmaf(g * g, K1, K0);
  float e = __builtin_amdgcn_exp2f(z);
  return g * __builtin_amdgcn_rcpf(1.0f + e) * u;
}
#define MFMA(a, b, c) __builtin_amdgcn_mfma_f32_32x32x16_bf16((a), (b), (c), 0, 0, 0)

struct Frag1 { s16x8 x, g[3], u[3]; };

__global__ __launch_bounds__(384, 2) void geglu_kernel(const char* __restrict__ x, const char* __restrict__ wg,
                                                       const char* __restrict__ wu, const char* __restrict__ wd,
                                                       char* __restrict__ out, char* __restrict__ ws
) {
  __shared__ __attribute__((aligned(16))) char smem[LDS_BYTES];
  const unsigned lds0 = (unsigned)(size_t)(LAS char*)smem;
  const int lane = threadIdx.x & 63;
  const int w = __builtin_amdgcn_readfirstlane(threadIdx.x >> 6);
  const int b = blockIdx.x, xcd = b & 7, bi = b >> 3, a = xcd & 3;
  const int p = (xcd >> 2) * 8 + (bi >> 2), j = a * 4 + (bi & 3);
  const int rh = a & 1, nt = (a >> 1) * 4 + (bi & 3);
  const int c0 = j < 14 ? 3 * j : 40;
  const int rot = (p + j) & 7;
  const bool prod = j < 15;
  const u64 tstart_ = __builtin_amdgcn_s_memrealtime();

  if (w >= 4) {
    const int rip = lane >> 3, pc = lane & 7;
    const unsigned dsrc = rip * 1024 + ((pc ^ (((rip >> 1) & 1) << 2)) << 4);
    const unsigned d0 = dsrc, d1 = dsrc + 7168, d2 = dsrc + 2 * 7168, d3 = dsrc + 3 * 7168;
    const char* wdb = wd + nt * 128;
    const int lw = w - 4;
    auto issue_wd = [&](int ps) {
#pragma unroll
      for (int g = 0; g < 5; ++g) { const int G = ps * 10 + 2 * g + lw; dma4(d0, d1, d2, d3, wdb + (size_t)G * 32768, lds0 + G * 4096); }
    };
    if (prod) {
      if (lw == 0) {
        const unsigned ve = rip * 1024 + ((pc ^ (rip >> 1)) << 4), vo = ve ^ 64;
        const unsigned v0 = ve, v1 = vo + 7168, v2 = ve + 2 * 7168, v3 = vo + 3 * 7168;
        const char* xb = x + (size_t)p * 131072;
        auto issue = [&](int s) {
          const int kc = (s + rot) & 7;
#pragma unroll
          for (int g = 0; g < 4; ++g) dma4(v0, v1, v2, v3, xb + g * 32768 + kc * 128, lds0 + (s & 3) * STAGE + g * 4096);
        };
        issue(0); issue(1);
        for (int s = 0; s < 6; ++s) { WAITV(16); BARRIER(); issue(s + 2); }
        WAITV(16); BARRIER(); issue_wd(0);
        WAITV(20); BARRIER(); issue_wd(1); issue_wd(2);
      } else {
        unsigned v[3];
#pragma unroll
        for (int i = 0; i < 3; ++i) { const int u = i * 64 + lane, row = u / 12, col = u - row * 12; v[i] = row * WPITCH + col * 16 - i * 1024; }
        const char* gb = wg + c0 * 64;
        const char* ub = wu + c0 * 64;
        auto issue = [&](int s) {
          const int kc = (s + rot) & 7;
#pragma unroll
          for (int g = 0; g < 4; ++g) {
            const size_t ro = (size_t)(kc * 64 + g * 16) * WPITCH;
            dma3(v[0], v[1], v[2], gb + ro, lds0 + (s & 3) * STAGE + 16384 + g * 3072);
            dma3(v[0], v[1], v[2], ub + ro, lds0 + (s & 3) * STAGE + 16384 + 12288 + g * 3072);
          }
        };
        issue(0); issue(1);
        for (int s = 0; s < 6; ++s) { WAITV(24); BARRIER(); issue(s + 2); }
        WAITV(24); BARRIER(); issue_wd(0);
        WAITV(20); BARRIER(); issue_wd(1); issue_wd(2);
      }
      STAMP(1);
      BARRIER();
      issue_wd(3);
    } else {
      issue_wd(0); issue_wd(1); issue_wd(2); issue_wd(3);
    }
    WAITV(0);
    STAMP(2);
    BARRIER();
    BARRIER();
    if (lw == 0) { dma4(d0, d1, d2, d3, wdb + (size_t)40 * 32768, lds0); dma4(d0, d1, d2, d3, wdb + (size_t)42 * 32768, lds0 + 8192); }
    else dma4(d0, d1, d2, d3, wdb + (size_t)41 * 32768, lds0 + 4096);
    WAITV(0);
    BARRIER();
    if (j == 15 && lw == 0) {
      GAS unsigned* ep = (GAS unsigned*)(ws + p * 256);
      const unsigned e0 = __hip_atomic_load(ep, __ATOMIC_RELAXED, __HIP_MEMORY_SCOPE_AGENT);
      const unsigned tag = e0 * 1664525u + 1013904223u;
      GAS unsigned* fl = (GAS unsigned*)(ws + WS_FLAG + p * 256) + lane;
      for (unsigned spin = 0; spin < SPIN_CAP; ++spin) {
        if (__all(__hip_atomic_load(fl, __ATOMIC_RELAXED, __HIP_MEMORY_SCOPE_AGENT) == tag)) break;
        __builtin_amdgcn_s_sleep(2);
      }
      if (lane == 0) __hip_atomic_store(ep, tag, __ATOMIC_RELAXED, __HIP_MEMORY_SCOPE_AGENT);
    }
    return;
  }

  const unsigned e0 = __hip_atomic_load((GAS unsigned*)(ws + p * 256), __ATOMIC_RELAXED, __HIP_MEMORY_SCOPE_AGENT);
  const int r = lane & 31, hh = lane >> 5, blk = (lane >> 4) & 1, q = (lane & 15) >> 2, pp = lane & 3;
  const __amdgpu_buffer_rsrc_t hrs = __builtin_amdgcn_make_buffer_rsrc(ws + WS_H, 0, H_BYTES, 0x00020000);

  if (prod) {
    f32x16 ag[3], au[3];
#pragma unroll
    for (int c = 0; c < 3; ++c) { ag[c] = (f32x16)(0.f); au[c] = (f32x16)(0.f); }
    const int xrow = w * 32 + r;
    const unsigned xa0 = lds0 + xrow * 128 + ((hh ^ ((xrow >> 1) & 7)) << 4);
    const unsigned wa0 = lds0 + 16384 + (8 * hh + q) * 192 + blk * 32 + pp * 8;
    auto load = [&](Frag1& f, unsigned sb, int ks) {
      f.x = *(LAS s16x8*)((xa0 ^ (ks * 32)) + sb);
      const unsigned wa = wa0 + sb + ks * 3072;
#pragma unroll
      for (int c = 0; c < 3; ++c) {
        f.g[c] = trfrag(wa + c * 64, wa + c * 64 + 768);
        f.u[c] = trfrag(wa + 12288 + c * 64, wa + 12288 + c * 64 + 768);
      }
    };
    auto mma = [&](const Frag1& f) {
#pragma unroll
      for (int c = 0; c < 3; ++c) { ag[c] = MFMA(f.g[c], f.x, ag[c]); au[c] = MFMA(f.u[c], f.x, au[c]); }
    };
    Frag1 f0, f1;
    BARRIER();
    while (__builtin_amdgcn_s_memrealtime() - tstart_ < 1000) {}
    load(f0, 0, 0);
    for (int s = 0; s < 8; ++s) {
      const unsigned sb = (s & 3) * STAGE;
      load(f1, sb, 1); mma(f0);
      load(f0, sb, 2); mma(f1);
      load(f1, sb, 3); mma(f0);
      BARRIER();
      if (s < 7) load(f0, ((s + 1) & 3) * STAGE, 0);
      mma(f1);
    }
    STAMP(2);
    const int hbase = ((p * 4 + w) * NCH + c0) * 2048 + lane * 16;
#pragma unroll
    for (int c = 0; c < 3; ++c) {
      if (j < 14 || c == 2) {
#pragma unroll
        for (int s = 0; s < 2; ++s) {
          u32x4 v;
#pragma unroll
          for (int i = 0; i < 4; ++i)
            v[i] = pack2(geglu(ag[c][8 * s + 2 * i], au[c][8 * s + 2 * i]), geglu(ag[c][8 * s + 2 * i + 1], au[c][8 * s + 2 * i + 1]));
          __builtin_amdgcn_raw_buffer_store_b128(v, hrs, hbase + c * 2048 + s * 1024, 0, 16);
        }
      }
    }
    STAMP(3);
    WAITV(0);
    STAMP(4);
  }
  const unsigned tag = e0 * 1664525u + 1013904223u;
  GAS unsigned* flags = (GAS unsigned*)(ws + WS_FLAG + p * 256);
  if (lane == 0) __hip_atomic_store(flags + w * 16 + j, tag, __ATOMIC_RELAXED, __HIP_MEMORY_SCOPE_AGENT);

  const int mh = w & 1, kh = w >> 1, mb = 2 * rh + mh;
  BARRIER();
  STAMP(5);
  for (unsigned spin = 0; spin < SPIN_CAP; ++spin) {
    if (__all(__hip_atomic_load(flags + mb * 16 + (lane & 15), __ATOMIC_RELAXED, __HIP_MEMORY_SCOPE_AGENT) == tag)) break;
    __builtin_amdgcn_s_sleep(1);
  }
  __builtin_amdgcn_fence(__ATOMIC_ACQUIRE, "wavefront");
  STAMP(6);
  u32x4 hf[22][2];
  {
    const int hb = ((p * 4 + mb) * NCH + kh) * 2048 + lane * 16;
#pragma unroll
    for (int i = 0; i < 22; ++i) {
      const int off = hb + (i < 21 ? i * 4096 : (kh ? 20 * 4096 : 21 * 4096));
      hf[i][0] = __builtin_amdgcn_raw_buffer_load_b128(hrs, off, 0, 16);
      hf[i][1] = __builtin_amdgcn_raw_buffer_load_b128(hrs, off + 1024, 0, 16);
    }
    __builtin_amdgcn_sched_barrier(0);
  }
  f32x16 o0 = (f32x16)(0.f), o1 = (f32x16)(0.f);
  const unsigned da0 = lds0 + kh * 4096 + (4 * hh + q) * 128 + ((q >> 1) << 6) + (16 * (pp & 1) + 8 * blk + 4 * (pp >> 1)) * 2;
  const unsigned da1 = da0 ^ 64;
#pragma unroll
  for (int i = 0; i < 22; ++i) {
    if (i == 2) BARRIER();
    if (i == 20) BARRIER();
    if (i < 21 || kh == 0) {
      const unsigned off = (i % 20) * 8192;
#pragma unroll
      for (int s = 0; s < 2; ++s) {
        const s16x8 a0 = trfrag(da0 + off + s * 2048, da0 + off + s * 2048 + 1024);
        const s16x8 a1 = trfrag(da1 + off + s * 2048, da1 + off + s * 2048 + 1024);
        const s16x8 hb = __builtin_bit_cast(s16x8, hf[i][s]);
        o0 = MFMA(a0, hb, o0);
        o1 = MFMA(a1, hb, o1);
      }
    }
  }
  STAMP(7);
  {
    LAS f32x4* xw = (LAS f32x4*)(size_t)(lds0 + 16384 + w * 4096 + lane * 16);
    const f32x16 snd = kh ? o0 : o1;
#pragma unroll
    for (int g = 0; g < 4; ++g) xw[g * 64] = (f32x4){snd[4 * g], snd[4 * g + 1], snd[4 * g + 2], snd[4 * g + 3]};
    BARRIER();
    LAS f32x4* xr = (LAS f32x4*)(size_t)(lds0 + 16384 + (w ^ 2) * 4096 + lane * 16);
    f32x16 kp = kh ? o1 : o0;
    u32x4 v[2];
#pragma unroll
    for (int g = 0; g < 4; ++g) {
      const f32x4 t = xr[g * 64];
      v[g >> 1][(g & 1) * 2] = pack2(kp[4 * g] + t[0], kp[4 * g + 1] + t[1]);
      v[g >> 1][(g & 1) * 2 + 1] = pack2(kp[4 * g + 2] + t[2], kp[4 * g + 3] + t[3]);
    }
    char* op = out + (size_t)(p * 128 + mb * 32 + r) * 1024 + nt * 128 + kh * 64 + hh * 32;
    *(GAS u32x4*)op = v[0];
    *(GAS u32x4*)(op + 16) = v[1];
  }
  STAMP(8);
}

extern "C" void kernel_launch(void* const* d_in, const int* in_sizes, int n_in, void* d_out, int out_size, void* d_ws,
                              size_t ws_size, hipStream_t stream) {
  geglu_kernel<<<256, 384, 0, stream>>>((const char*)d_in[0], (const char*)d_in[1], (const char*)d_in[2],
                                        (const char*)d_in[3], (char*)d_out, (char*)d_ws);
}
```

```cpp
#include <hip/hip_runtime.h>
#include <hip/hip_bf16.h>

typedef short s16x4 __attribute__((ext_vector_type(4)));
typedef short s16x8 __attribute__((ext_vector_type(8)));
typedef float f32x2 __attribute__((ext_vector_type(2)));
typedef float f32x4 __attribute__((ext_vector_type(4)));
typedef float f32x16 __attribute__((ext_vector_type(16)));
typedef unsigned u32x4 __attribute__((ext_vector_type(4)));
typedef __bf16 bf16x2 __attribute__((ext_vector_type(2)));
typedef unsigned long long u64;
#define LAS __attribute__((address_space(3)))
#define GAS __attribute__((address_space(1)))
#define DI __device__ __forceinline__

constexpr int S = 2048, DM = 512, DF = 1376;
constexpr int NCH = 43;
constexpr int WPITCH = DF * 2;
constexpr int STAGE = 40960;
constexpr int LDS_BYTES = 163840;
constexpr int WS_FLAG = 4096;
constexpr int WS_H = 65536;
constexpr int H_BYTES = 64 * NCH * 2048;
constexpr unsigned SPIN_CAP = 1u << 16;

#define STAMP(i) do {} while (0)

#define BARRIER() asm volatile("s_waitcnt lgkmcnt(0)\n\ts_barrier" ::: "memory")
#define WAITV(n) asm volatile("s_waitcnt vmcnt(" #n ")" ::: "memory")

DI void dma4(unsigned v0, unsigned v1, unsigned v2, unsigned v3, const char* sbase, unsigned lds) {
  asm volatile("s_mov_b32 m0, %5\n\ts_nop 0\n\t"
               "global_load_lds_dwordx4 %0, %4\n\t"
               "global_load_lds_dwordx4 %1, %4 offset:1024\n\t"
               "global_load_lds_dwordx4 %2, %4 offset:2048\n\t"
               "global_load_lds_dwordx4 %3, %4 offset:3072"
               ::"v"(v0), "v"(v1), "v"(v2), "v"(v3), "s"(sbase), "s"(lds) : "memory");
}
DI void dma3(unsigned v0, unsigned v1, unsigned v2, const char* sbase, unsigned lds) {
  asm volatile("s_mov_b32 m0, %4\n\ts_nop 0\n\t"
               "global_load_lds_dwordx4 %0, %3\n\t"
               "global_load_lds_dwordx4 %1, %3 offset:1024\n\t"
               "global_load_lds_dwordx4 %2, %3 offset:2048"
               ::"v"(v0), "v"(v1), "v"(v2), "s"(sbase), "s"(lds) : "memory");
}
DI s16x8 trfrag(unsigned a0, unsigned a1) {
  s16x4 lo = __builtin_amdgcn_ds_read_tr16_b64_v4i16((LAS s16x4*)a0);
  s16x4 hi = __builtin_amdgcn_ds_read_tr16_b64_v4i16((LAS s16x4*)a1);
  return __builtin_shufflevector(lo, hi, 0, 1, 2, 3, 4, 5, 6, 7);
}
DI unsigned pack2(float a, float b) {
  f32x2 v = {a, b};
  return __builtin_bit_cast(unsigned, __builtin_convertvector(v, bf16x2));
}
DI float geglu(float g, float u) {
  constexpr float K0 = -2.0f * 0.7978845608028654f * 1.4426950408889634f;
  constexpr float K1 = K0 * 0.044715f;
  float z = g * __builtin_fmaf(g * g, K1, K0);
  float e = __builtin_amdgcn_exp2f(z);
  return g * __builtin_amdgcn_rcpf(1.0f + e) * u;
}
#define MFMA(a, b, c) __builtin_amdgcn_mfma_f32_32x32x16_bf16((a), (b), (c), 0, 0, 0)

struct Frag1 { s16x8 x, g[3], u[3]; };

__global__ __launch_bounds__(384, 2) void geglu_kernel(const char* __restrict__ x, const char* __restrict__ wg,
                                                       const char* __restrict__ wu, const char* __restrict__ wd,
                                                       char* __restrict__ out, char* __restrict__ ws
) {
  __shared__ __attribute__((aligned(16))) char smem[LDS_BYTES];
  const unsigned lds0 = (unsigned)(size_t)(LAS char*)smem;
  const int lane = threadIdx.x & 63;
  const int w = __builtin_amdgcn_readfirstlane(threadIdx.x >> 6);
  const int b = blockIdx.x, xcd = b & 7, bi = b >> 3, a = xcd & 3;
  const int p = (xcd >> 2) * 8 + (bi >> 2), j = a * 4 + (bi & 3);
  const int rh = a & 1, nt = (a >> 1) * 4 + (bi & 3);
  const int c0 = j < 14 ? 3 * j : 40;
  const int rot = (p + j) & 7;
  const bool prod = j < 15;
  const u64 tstart_ = __hip_atomic_load((GAS u64*)(ws + 32768), __ATOMIC_RELAXED, __HIP_MEMORY_SCOPE_AGENT);

  if (w >= 4) {
    const int rip = lane >> 3, pc = lane & 7;
    const unsigned dsrc = rip * 1024 + ((pc ^ (((rip >> 1) & 1) << 2)) << 4);
    const unsigned d0 = dsrc, d1 = dsrc + 7168, d2 = dsrc + 2 * 7168, d3 = dsrc + 3 * 7168;
    const char* wdb = wd + nt * 128;
    const int lw = w - 4;
    auto issue_wd = [&](int ps) {
#pragma unroll
      for (int g = 0; g < 5; ++g) { const int G = ps * 10 + 2 * g + lw; dma4(d0, d1, d2, d3, wdb + (size_t)G * 32768, lds0 + G * 4096); }
    };
    if (prod) {
      if (lw == 0) {
        const unsigned ve = rip * 1024 + ((pc ^ (rip >> 1)) << 4), vo = ve ^ 64;
        const unsigned v0 = ve, v1 = vo + 7168, v2 = ve + 2 * 7168, v3 = vo + 3 * 7168;
        const char* xb = x + (size_t)p * 131072;
        auto issue = [&](int s) {
          const int kc = (s + rot) & 7;
#pragma unroll
          for (int g = 0; g < 4; ++g) dma4(v0, v1, v2, v3, xb + g * 32768 + kc * 128, lds0 + (s & 3) * STAGE + g * 4096);
        };
        issue(0); issue(1);
        for (int s = 0; s < 6; ++s) { WAITV(16); BARRIER(); issue(s + 2); }
        WAITV(16); BARRIER(); issue_wd(0);
        WAITV(20); BARRIER(); issue_wd(1); issue_wd(2);
      } else {
        unsigned v[3];
#pragma unroll
        for (int i = 0; i < 3; ++i) { const int u = i * 64 + lane, row = u / 12, col = u - row * 12; v[i] = row * WPITCH + col * 16 - i * 1024; }
        const char* gb = wg + c0 * 64;
        const char* ub = wu + c0 * 64;
        auto issue = [&](int s) {
          const int kc = (s + rot) & 7;
#pragma unroll
          for (int g = 0; g < 4; ++g) {
            const size_t ro = (size_t)(kc * 64 + g * 16) * WPITCH;
            dma3(v[0], v[1], v[2], gb + ro, lds0 + (s & 3) * STAGE + 16384 + g * 3072);
            dma3(v[0], v[1], v[2], ub + ro, lds0 + (s & 3) * STAGE + 16384 + 12288 + g * 3072);
          }
        };
        issue(0); issue(1);
        for (int s = 0; s < 6; ++s) { WAITV(24); BARRIER(); issue(s + 2); }
        WAITV(24); BARRIER(); issue_wd(0);
        WAITV(20); BARRIER(); issue_wd(1); issue_wd(2);
      }
      STAMP(1);
      BARRIER();
      issue_wd(3);
    } else {
      issue_wd(0); issue_wd(1); issue_wd(2); issue_wd(3);
    }
    WAITV(0);
    STAMP(2);
    BARRIER();
    BARRIER();
    if (lw == 0) { dma4(d0, d1, d2, d3, wdb + (size_t)40 * 32768, lds0); dma4(d0, d1, d2, d3, wdb + (size_t)42 * 32768, lds0 + 8192); }
    else dma4(d0, d1, d2, d3, wdb + (size_t)41 * 32768, lds0 + 4096);
    WAITV(0);
    BARRIER();
    if (j == 15 && lw == 0) {
      GAS unsigned* ep = (GAS unsigned*)(ws + p * 256);
      const unsigned e0 = __hip_atomic_load(ep, __ATOMIC_RELAXED, __HIP_MEMORY_SCOPE_AGENT);
      const unsigned tag = e0 * 1664525u + 1013904223u;
      GAS unsigned* fl = (GAS unsigned*)(ws + WS_FLAG + p * 256) + lane;
      for (unsigned spin = 0; spin < SPIN_CAP; ++spin) {
        if (__all(__hip_atomic_load(fl, __ATOMIC_RELAXED, __HIP_MEMORY_SCOPE_AGENT) == tag)) break;
        __builtin_amdgcn_s_sleep(2);
      }
      if (lane == 0) __hip_atomic_store(ep, tag, __ATOMIC_RELAXED, __HIP_MEMORY_SCOPE_AGENT);
    }
    return;
  }

  const unsigned e0 = __hip_atomic_load((GAS unsigned*)(ws + p * 256), __ATOMIC_RELAXED, __HIP_MEMORY_SCOPE_AGENT);
  const int r = lane & 31, hh = lane >> 5, blk = (lane >> 4) & 1, q = (lane & 15) >> 2, pp = lane & 3;
  const __amdgpu_buffer_rsrc_t hrs = __builtin_amdgcn_make_buffer_rsrc(ws + WS_H, 0, H_BYTES, 0x00020000);

  if (prod) {
    f32x16 ag[3], au[3];
#pragma unroll
    for (int c = 0; c < 3; ++c) { ag[c] = (f32x16)(0.f); au[c] = (f32x16)(0.f); }
    const int xrow = w * 32 + r;
    const unsigned xa0 = lds0 + xrow * 128 + ((hh ^ ((xrow >> 1) & 7)) << 4);
    const unsigned wa0 = lds0 + 16384 + (8 * hh + q) * 192 + blk * 32 + pp * 8;
    auto load = [&](Frag1& f, unsigned sb, int ks) {
      f.x = *(LAS s16x8*)((xa0 ^ (ks * 32)) + sb);
      const unsigned wa = wa0 + sb + ks * 3072;
#pragma unroll
      for (int c = 0; c < 3; ++c) {
        f.g[c] = trfrag(wa + c * 64, wa + c * 64 + 768);
        f.u[c] = trfrag(wa + 12288 + c * 64, wa + 12288 + c * 64 + 768);
      }
    };
    auto mma = [&](const Frag1& f) {
#pragma unroll
      for (int c = 0; c < 3; ++c) { ag[c] = MFMA(f.g[c], f.x, ag[c]); au[c] = MFMA(f.u[c], f.x, au[c]); }
    };
    Frag1 f0, f1;
    BARRIER();
    STAMP(1);
    load(f0, 0, 0);
    for (int s = 0; s < 8; ++s) {
      const unsigned sb = (s & 3) * STAGE;
      load(f1, sb, 1); mma(f0);
      load(f0, sb, 2); mma(f1);
      load(f1, sb, 3); mma(f0);
      BARRIER();
      if (s < 7) load(f0, ((s + 1) & 3) * STAGE, 0);
      mma(f1);
    }
    STAMP(2);
    const int hbase = ((p * 4 + w) * NCH + c0) * 2048 + lane * 16;
#pragma unroll
    for (int c = 0; c < 3; ++c) {
      if (j < 14 || c == 2) {
#pragma unroll
        for (int s = 0; s < 2; ++s) {
          u32x4 v;
#pragma unroll
          for (int i = 0; i < 4; ++i)
            v[i] = pack2(geglu(ag[c][8 * s + 2 * i], au[c][8 * s + 2 * i]), geglu(ag[c][8 * s + 2 * i + 1], au[c][8 * s + 2 * i + 1]));
          __builtin_amdgcn_raw_buffer_store_b128(v, hrs, hbase + c * 2048 + s * 1024, 0, 16);
        }
      }
    }
    STAMP(3);
    WAITV(0);
    STAMP(4);
  }
  const unsigned tag = e0 * 1664525u + 1013904223u;
  GAS unsigned* flags = (GAS unsigned*)(ws + WS_FLAG + p * 256);
  if (lane == 0) __hip_atomic_store(flags + w * 16 + j, tag, __ATOMIC_RELAXED, __HIP_MEMORY_SCOPE_AGENT);

  const int mh = w & 1, kh = w >> 1, mb = 2 * rh + mh;
  BARRIER();
  STAMP(5);
  for (unsigned spin = 0; spin < SPIN_CAP; ++spin) {
    if (__all(__hip_atomic_load(flags + mb * 16 + (lane & 15), __ATOMIC_RELAXED, __HIP_MEMORY_SCOPE_AGENT) == tag)) break;
    __builtin_amdgcn_s_sleep(1);
  }
  __builtin_amdgcn_fence(__ATOMIC_ACQUIRE, "wavefront");
  STAMP(6);
  u32x4 hf[22][2];
  {
    const int hb = ((p * 4 + mb) * NCH + kh) * 2048 + lane * 16;
#pragma unroll
    for (int i = 0; i < 22; ++i) {
      const int off = hb + (i < 21 ? i * 4096 : (kh ? 20 * 4096 : 21 * 4096));
      hf[i][0] = __builtin_amdgcn_raw_buffer_load_b128(hrs, off, 0, 16);
      hf[i][1] = __builtin_amdgcn_raw_buffer_load_b128(hrs, off + 1024, 0, 16);
    }
    __builtin_amdgcn_sched_barrier(0);
  }
  f32x16 o0 = (f32x16)(0.f), o1 = (f32x16)(0.f);
  const unsigned da0 = lds0 + kh * 4096 + (4 * hh + q) * 128 + ((q >> 1) << 6) + (16 * (pp & 1) + 8 * blk + 4 * (pp >> 1)) * 2;
  const unsigned da1 = da0 ^ 64;
#pragma unroll
  for (int i = 0; i < 22; ++i) {
    if (i == 2) BARRIER();
    if (i == 20) BARRIER();
    if (i < 21 || kh == 0) {
      const unsigned off = (i % 20) * 8192;
#pragma unroll
      for (int s = 0; s < 2; ++s) {
        const s16x8 a0 = trfrag(da0 + off + s * 2048, da0 + off + s * 2048 + 1024);
        const s16x8 a1 = trfrag(da1 + off + s * 2048, da1 + off + s * 2048 + 1024);
        const s16x8 hb = __builtin_bit_cast(s16x8, hf[i][s]);
        o0 = MFMA(a0, hb, o0);
        o1 = MFMA(a1, hb, o1);
      }
    }
  }
  STAMP(7);
  {
    LAS f32x4* xw = (LAS f32x4*)(size_t)(lds0 + 16384 + w * 4096 + lane * 16);
    const f32x16 snd = kh ? o0 : o1;
#pragma unroll
    for (int g = 0; g < 4; ++g) xw[g * 64] = (f32x4){snd[4 * g], snd[4 * g + 1], snd[4 * g + 2], snd[4 * g + 3]};
    BARRIER();
    LAS f32x4* xr = (LAS f32x4*)(size_t)(lds0 + 16384 + (w ^ 2) * 4096 + lane * 16);
    f32x16 kp = kh ? o1 : o0;
    u32x4 v[2];
#pragma unroll
    for (int g = 0; g < 4; ++g) {
      const f32x4 t = xr[g * 64];
      v[g >> 1][(g & 1) * 2] = pack2(kp[4 * g] + t[0], kp[4 * g + 1] + t[1]);
      v[g >> 1][(g & 1) * 2 + 1] = pack2(kp[4 * g + 2] + t[2], kp[4 * g + 3] + t[3]);
    }
    char* op = out + (size_t)(p * 128 + mb * 32 + r) * 1024 + nt * 128 + kh * 64 + hh * 32;
    { const __amdgpu_buffer_rsrc_t ors = __builtin_amdgcn_make_buffer_rsrc(out, 0, 2097152, 0x00020000);
      const int oo = (int)(op - out);
      __builtin_amdgcn_raw_buffer_store_b128(v[0], ors, oo, 0, 17);
      __builtin_amdgcn_raw_buffer_store_b128(v[1], ors, oo + 16, 0, 17); }
  }
  while (__builtin_amdgcn_s_memrealtime() - tstart_ < 4000) {}
}

__global__ void tiny_k(char* ws) { if (threadIdx.x == 0) *(u64*)(ws + 32768) = __builtin_amdgcn_s_memrealtime(); }
extern "C" void kernel_launch(void* const* d_in, const int* in_sizes, int n_in, void* d_out, int out_size, void* d_ws,
                              size_t ws_size, hipStream_t stream) {
  tiny_k<<<1, 64, 0, stream>>>((char*)d_ws);
  geglu_kernel<<<256, 384, 0, stream>>>((const char*)d_in[0], (const char*)d_in[1], (const char*)d_in[2],
                                        (const char*)d_in[3], (char*)d_out, (char*)d_ws);
}
```

```cpp
#include <hip/hip_runtime.h>
#include <hip/hip_bf16.h>

typedef short s16x4 __attribute__((ext_vector_type(4)));
typedef short s16x8 __attribute__((ext_vector_type(8)));
typedef float f32x2 __attribute__((ext_vector_type(2)));
typedef float f32x4 __attribute__((ext_vector_type(4)));
typedef float f32x16 __attribute__((ext_vector_type(16)));
typedef unsigned u32x4 __attribute__((ext_vector_type(4)));
typedef __bf16 bf16x2 __attribute__((ext_vector_type(2)));
typedef unsigned long long u64;
#define LAS __attribute__((address_space(3)))
#define GAS __attribute__((address_space(1)))
#define DI __device__ __forceinline__

constexpr int S = 2048, DM = 512, DF = 1376;
constexpr int NCH = 43;
constexpr int WPITCH = DF * 2;
constexpr int STAGE = 40960;
constexpr int LDS_BYTES = 163840;
constexpr int WS_FLAG = 4096;
constexpr int WS_H = 65536;
constexpr int H_BYTES = 64 * NCH * 2048;
constexpr unsigned SPIN_CAP = 1u << 16;

#define STAMP(i) do {} while (0)

#define BARRIER() asm volatile("s_waitcnt lgkmcnt(0)\n\ts_barrier" ::: "memory")
#define WAITV(n) asm volatile("s_waitcnt vmcnt(" #n ")" ::: "memory")

DI void dma4(unsigned v0, unsigned v1, unsigned v2, unsigned v3, const char* sbase, unsigned lds) {
  asm volatile("s_mov_b32 m0, %5\n\ts_nop 0\n\t"
               "global_load_lds_dwordx4 %0, %4\n\t"
               "global_load_lds_dwordx4 %1, %4 offset:1024\n\t"
               "global_load_lds_dwordx4 %2, %4 offset:2048\n\t"
               "global_load_lds_dwordx4 %3, %4 offset:3072"
               ::"v"(v0), "v"(v1), "v"(v2), "v"(v3), "s"(sbase), "s"(lds) : "memory");
}
DI void dma3(unsigned v0, unsigned v1, unsigned v2, const char* sbase, unsigned lds) {
  asm volatile("s_mov_b32 m0, %4\n\ts_nop 0\n\t"
               "global_load_lds_dwordx4 %0, %3\n\t"
               "global_load_lds_dwordx4 %1, %3 offset:1024\n\t"
               "global_load_lds_dwordx4 %2, %3 offset:2048"
               ::"v"(v0), "v"(v1), "v"(v2), "s"(sbase), "s"(lds) : "memory");
}
DI s16x8 trfrag(unsigned a0, unsigned a1) {
  s16x4 lo = __builtin_amdgcn_ds_read_tr16_b64_v4i16((LAS s16x4*)a0);
  s16x4 hi = __builtin_amdgcn_ds_read_tr16_b64_v4i16((LAS s16x4*)a1);
  return __builtin_shufflevector(lo, hi, 0, 1, 2, 3, 4, 5, 6, 7);
}
DI unsigned pack2(float a, float b) {
  f32x2 v = {a, b};
  return __builtin_bit_cast(unsigned, __builtin_convertvector(v, bf16x2));
}
DI float geglu(float g, float u) {
  constexpr float K0 = -2.0f * 0.7978845608028654f * 1.4426950408889634f;
  constexpr float K1 = K0 * 0.044715f;
  float z = g * __builtin_fmaf(g * g, K1, K0);
  float e = __builtin_amdgcn_exp2f(z);
  return g * __builtin_amdgcn_rcpf(1.0f + e) * u;
}
#define MFMA(a, b, c) __builtin_amdgcn_mfma_f32_32x32x16_bf16((a), (b), (c), 0, 0, 0)

struct Frag1 { s16x8 x, g[3], u[3]; };

__global__ __launch_bounds__(384, 2) void geglu_kernel(const char* __restrict__ x, const char* __restrict__ wg,
                                                       const char* __restrict__ wu, const char* __restrict__ wd,
                                                       char* __restrict__ out, char* __restrict__ ws
) {
  __shared__ __attribute__((aligned(16))) char smem[LDS_BYTES];
  const unsigned lds0 = (unsigned)(size_t)(LAS char*)smem;
  const int lane = threadIdx.x & 63;
  const int w = __builtin_amdgcn_readfirstlane(threadIdx.x >> 6);
  const int b = blockIdx.x, xcd = b & 7, bi = b >> 3, a = xcd & 3;
  const int p = (xcd >> 2) * 8 + (bi >> 2), j = a * 4 + (bi & 3);
  const int rh = a & 1, nt = (a >> 1) * 4 + (bi & 3);
  const int c0 = j < 14 ? 3 * j : 40;
  const int rot = (p + j) & 7;
  const bool prod = j < 15;
  if (blockIdx.x == 0 && threadIdx.x == 0) *(GAS u64*)(ws + 32768) = __builtin_amdgcn_s_memrealtime();

  if (w >= 4) {
    const int rip = lane >> 3, pc = lane & 7;
    const unsigned dsrc = rip * 1024 + ((pc ^ (((rip >> 1) & 1) << 2)) << 4);
    const unsigned d0 = dsrc, d1 = dsrc + 7168, d2 = dsrc + 2 * 7168, d3 = dsrc + 3 * 7168;
    const char* wdb = wd + nt * 128;
    const int lw = w - 4;
    auto issue_wd = [&](int ps) {
#pragma unroll
      for (int g = 0; g < 5; ++g) { const int G = ps * 10 + 2 * g + lw; dma4(d0, d1, d2, d3, wdb + (size_t)G * 32768, lds0 + G * 4096); }
    };
    if (prod) {
      if (lw == 0) {
        const unsigned ve = rip * 1024 + ((pc ^ (rip >> 1)) << 4), vo = ve ^ 64;
        const unsigned v0 = ve, v1 = vo + 7168, v2 = ve + 2 * 7168, v3 = vo + 3 * 7168;
        const char* xb = x + (size_t)p * 131072;
        auto issue = [&](int s) {
          const int kc = (s + rot) & 7;
#pragma unroll
          for (int g = 0; g < 4; ++g) dma4(v0, v1, v2, v3, xb + g * 32768 + kc * 128, lds0 + (s & 3) * STAGE + g * 4096);
        };
        issue(0); issue(1);
        for (int s = 0; s < 6; ++s) { WAITV(16); BARRIER(); issue(s + 2); }
        WAITV(16); BARRIER(); issue_wd(0);
        WAITV(20); BARRIER(); issue_wd(1); issue_wd(2);
      } else {
        unsigned v[3];
#pragma unroll
        for (int i = 0; i < 3; ++i) { const int u = i * 64 + lane, row = u / 12, col = u - row * 12; v[i] = row * WPITCH + col * 16 - i * 1024; }
        const char* gb = wg + c0 * 64;
        const char* ub = wu + c0 * 64;
        auto issue = [&](int s) {
          const int kc = (s + rot) & 7;
#pragma unroll
          for (int g = 0; g < 4; ++g) {
            const size_t ro = (size_t)(kc * 64 + g * 16) * WPITCH;
            dma3(v[0], v[1], v[2], gb + ro, lds0 + (s & 3) * STAGE + 16384 + g * 3072);
            dma3(v[0], v[1], v[2], ub + ro, lds0 + (s & 3) * STAGE + 16384 + 12288 + g * 3072);
          }
        };
        issue(0); issue(1);
        for (int s = 0; s < 6; ++s) { WAITV(24); BARRIER(); issue(s + 2); }
        WAITV(24); BARRIER(); issue_wd(0);
        WAITV(20); BARRIER(); issue_wd(1); issue_wd(2);
      }
      STAMP(1);
      BARRIER();
      issue_wd(3);
    } else {
      issue_wd(0); issue_wd(1); issue_wd(2); issue_wd(3);
    }
    WAITV(0);
    STAMP(2);
    BARRIER();
    BARRIER();
    if (lw == 0) { dma4(d0, d1, d2, d3, wdb + (size_t)40 * 32768, lds0); dma4(d0, d1, d2, d3, wdb + (size_t)42 * 32768, lds0 + 8192); }
    else dma4(d0, d1, d2, d3, wdb + (size_t)41 * 32768, lds0 + 4096);
    WAITV(0);
    BARRIER();
    if (j == 15 && lw == 0) {
      GAS unsigned* ep = (GAS unsigned*)(ws + p * 256);
      const unsigned e0 = __hip_atomic_load(ep, __ATOMIC_RELAXED, __HIP_MEMORY_SCOPE_AGENT);
      const unsigned tag = e0 * 1664525u + 1013904223u;
      GAS unsigned* fl = (GAS unsigned*)(ws + WS_FLAG + p * 256) + lane;
      for (unsigned spin = 0; spin < SPIN_CAP; ++spin) {
        if (__all(__hip_atomic_load(fl, __ATOMIC_RELAXED, __HIP_MEMORY_SCOPE_AGENT) == tag)) break;
        __builtin_amdgcn_s_sleep(2);
      }
      if (lane == 0) __hip_atomic_store(ep, tag, __ATOMIC_RELAXED, __HIP_MEMORY_SCOPE_AGENT);
    }
    return;
  }

  const unsigned e0 = __hip_atomic_load((GAS unsigned*)(ws + p * 256), __ATOMIC_RELAXED, __HIP_MEMORY_SCOPE_AGENT);
  const int r = lane & 31, hh = lane >> 5, blk = (lane >> 4) & 1, q = (lane & 15) >> 2, pp = lane & 3;
  const __amdgpu_buffer_rsrc_t hrs = __builtin_amdgcn_make_buffer_rsrc(ws + WS_H, 0, H_BYTES, 0x00020000);

  if (prod) {
    f32x16 ag[3], au[3];
#pragma unroll
    for (int c = 0; c < 3; ++c) { ag[c] = (f32x16)(0.f); au[c] = (f32x16)(0.f); }
    const int xrow = w * 32 + r;
    const unsigned xa0 = lds0 + xrow * 128 + ((hh ^ ((xrow >> 1) & 7)) << 4);
    const unsigned wa0 = lds0 + 16384 + (8 * hh + q) * 192 + blk * 32 + pp * 8;
    auto load = [&](Frag1& f, unsigned sb, int ks) {
      f.x = *(LAS s16x8*)((xa0 ^ (ks * 32)) + sb);
      const unsigned wa = wa0 + sb + ks * 3072;
#pragma unroll
      for (int c = 0; c < 3; ++c) {
        f.g[c] = trfrag(wa + c * 64, wa + c * 64 + 768);
        f.u[c] = trfrag(wa + 12288 + c * 64, wa + 12288 + c * 64 + 768);
      }
    };
    auto mma = [&](const Frag1& f) {
#pragma unroll
      for (int c = 0; c < 3; ++c) { ag[c] = MFMA(f.g[c], f.x, ag[c]); au[c] = MFMA(f.u[c], f.x, au[c]); }
    };
    Frag1 f0, f1;
    BARRIER();
    STAMP(1);
    load(f0, 0, 0);
    for (int s = 0; s < 8; ++s) {
      const unsigned sb = (s & 3) * STAGE;
      load(f1, sb, 1); mma(f0);
      load(f0, sb, 2); mma(f1);
      load(f1, sb, 3); mma(f0);
      BARRIER();
      if (s < 7) load(f0, ((s + 1) & 3) * STAGE, 0);
      mma(f1);
    }
    STAMP(2);
    const int hbase = ((p * 4 + w) * NCH + c0) * 2048 + lane * 16;
#pragma unroll
    for (int c = 0; c < 3; ++c) {
      if (j < 14 || c == 2) {
#pragma unroll
        for (int s = 0; s < 2; ++s) {
          u32x4 v;
#pragma unroll
          for (int i = 0; i < 4; ++i)
            v[i] = pack2(geglu(ag[c][8 * s + 2 * i], au[c][8 * s + 2 * i]), geglu(ag[c][8 * s + 2 * i + 1], au[c][8 * s + 2 * i + 1]));
          __builtin_amdgcn_raw_buffer_store_b128(v, hrs, hbase + c * 2048 + s * 1024, 0, 16);
        }
      }
    }
    STAMP(3);
    WAITV(0);
    STAMP(4);
  }
  const unsigned tag = e0 * 1664525u + 1013904223u;
  GAS unsigned* flags = (GAS unsigned*)(ws + WS_FLAG + p * 256);
  if (lane == 0) __hip_atomic_store(flags + w * 16 + j, tag, __ATOMIC_RELAXED, __HIP_MEMORY_SCOPE_AGENT);

  const int mh = w & 1, kh = w >> 1, mb = 2 * rh + mh;
  BARRIER();
  STAMP(5);
  for (unsigned spin = 0; spin < SPIN_CAP; ++spin) {
    if (__all(__hip_atomic_load(flags + mb * 16 + (lane & 15), __ATOMIC_RELAXED, __HIP_MEMORY_SCOPE_AGENT) == tag)) break;
    __builtin_amdgcn_s_sleep(1);
  }
  __builtin_amdgcn_fence(__ATOMIC_ACQUIRE, "wavefront");
  STAMP(6);
  u32x4 hf[22][2];
  {
    const int hb = ((p * 4 + mb) * NCH + kh) * 2048 + lane * 16;
#pragma unroll
    for (int i = 0; i < 22; ++i) {
      const int off = hb + (i < 21 ? i * 4096 : (kh ? 20 * 4096 : 21 * 4096));
      hf[i][0] = __builtin_amdgcn_raw_buffer_load_b128(hrs, off, 0, 16);
      hf[i][1] = __builtin_amdgcn_raw_buffer_load_b128(hrs, off + 1024, 0, 16);
    }
    __builtin_amdgcn_sched_barrier(0);
  }
  f32x16 o0 = (f32x16)(0.f), o1 = (f32x16)(0.f);
  const unsigned da0 = lds0 + kh * 4096 + (4 * hh + q) * 128 + ((q >> 1) << 6) + (16 * (pp & 1) + 8 * blk + 4 * (pp >> 1)) * 2;
  const unsigned da1 = da0 ^ 64;
#pragma unroll
  for (int i = 0; i < 22; ++i) {
    if (i == 2) BARRIER();
    if (i == 20) BARRIER();
    if (i < 21 || kh == 0) {
      const unsigned off = (i % 20) * 8192;
#pragma unroll
      for (int s = 0; s < 2; ++s) {
        const s16x8 a0 = trfrag(da0 + off + s * 2048, da0 + off + s * 2048 + 1024);
        const s16x8 a1 = trfrag(da1 + off + s * 2048, da1 + off + s * 2048 + 1024);
        const s16x8 hb = __builtin_bit_cast(s16x8, hf[i][s]);
        o0 = MFMA(a0, hb, o0);
        o1 = MFMA(a1, hb, o1);
      }
    }
  }
  STAMP(7);
  {
    LAS f32x4* xw = (LAS f32x4*)(size_t)(lds0 + 16384 + w * 4096 + lane * 16);
    const f32x16 snd = kh ? o0 : o1;
#pragma unroll
    for (int g = 0; g < 4; ++g) xw[g * 64] = (f32x4){snd[4 * g], snd[4 * g + 1], snd[4 * g + 2], snd[4 * g + 3]};
    BARRIER();
    LAS f32x4* xr = (LAS f32x4*)(size_t)(lds0 + 16384 + (w ^ 2) * 4096 + lane * 16);
    f32x16 kp = kh ? o1 : o0;
    u32x4 v[2];
#pragma unroll
    for (int g = 0; g < 4; ++g) {
      const f32x4 t = xr[g * 64];
      v[g >> 1][(g & 1) * 2] = pack2(kp[4 * g] + t[0], kp[4 * g + 1] + t[1]);
      v[g >> 1][(g & 1) * 2 + 1] = pack2(kp[4 * g + 2] + t[2], kp[4 * g + 3] + t[3]);
    }
    char* op = out + (size_t)(p * 128 + mb * 32 + r) * 1024 + nt * 128 + kh * 64 + hh * 32;
    *(GAS u32x4*)op = v[0];
    *(GAS u32x4*)(op + 16) = v[1];
  }
  STAMP(8);
}

__global__ void tail_k(char* ws) {

  const u64 t = *(volatile u64*)(ws + 32768);
  while (__builtin_amdgcn_s_memrealtime() - t < 4000) { __builtin_amdgcn_s_sleep(1); }

}
extern "C" void kernel_launch(void* const* d_in, const int* in_sizes, int n_in, void* d_out, int out_size, void* d_ws,
                              size_t ws_size, hipStream_t stream) {
  geglu_kernel<<<256, 384, 0, stream>>>((const char*)d_in[0], (const char*)d_in[1], (const char*)d_in[2],
                                        (const char*)d_in[3], (char*)d_out, (char*)d_ws);
  tail_k<<<1, 64, 0, stream>>>((char*)d_ws);
}
```

```cpp
#include <hip/hip_runtime.h>
#include <hip/hip_bf16.h>

typedef short s16x4 __attribute__((ext_vector_type(4)));
typedef short s16x8 __attribute__((ext_vector_type(8)));
typedef float f32x2 __attribute__((ext_vector_type(2)));
typedef float f32x4 __attribute__((ext_vector_type(4)));
typedef unsigned u32x4 __attribute__((ext_vector_type(4)));
typedef __bf16 bf16x2 __attribute__((ext_vector_type(2)));
typedef unsigned long long u64;
#define LAS __attribute__((address_space(3)))
#define GAS __attribute__((address_space(1)))
#define DI __device__ __forceinline__

constexpr int S = 2048, DM = 512, DF = 1376;
constexpr int NCH = 43;
constexpr int WPITCH = DF * 2;
constexpr int STAGE = 40960;
constexpr int LDS_BYTES = 163840;
constexpr int MBOX = 256;
constexpr int WS_H = 65536;
constexpr int H_BYTES = 64 * NCH * 2048;
constexpr unsigned SPIN_CAP = 1u << 16;
constexpr unsigned TAG_STEP = 0x9E3779B9u;

#define BAR() asm volatile("s_barrier" ::: "memory")
#define BAR_L() asm volatile("s_waitcnt lgkmcnt(0)\n\ts_barrier" ::: "memory")
#define WAITV(n) asm volatile("s_waitcnt vmcnt(%0)" ::"i"(n) : "memory")
#define FENCE() __builtin_amdgcn_sched_barrier(0)
#define OPAQUE_V(v_) asm volatile("" : "+v"(v_))

DI void dma4(unsigned v0, unsigned v1, unsigned v2, unsigned v3, const char* sbase, unsigned lds) {
  asm volatile("s_mov_b32 m0, %5\n\ts_nop 0\n\t"
               "global_load_lds_dwordx4 %0, %4\n\t"
               "global_load_lds_dwordx4 %1, %4 offset:1024\n\t"
               "global_load_lds_dwordx4 %2, %4 offset:2048\n\t"
               "global_load_lds_dwordx4 %3, %4 offset:3072"
               ::"v"(v0), "v"(v1), "v"(v2), "v"(v3), "s"(sbase), "s"(lds) : "memory");
}
DI void dma3(unsigned v0, unsigned v1, unsigned v2, const char* sbase, unsigned lds) {
  asm volatile("s_mov_b32 m0, %4\n\ts_nop 0\n\t"
               "global_load_lds_dwordx4 %0, %3\n\t"
               "global_load_lds_dwordx4 %1, %3 offset:1024\n\t"
               "global_load_lds_dwordx4 %2, %3 offset:2048"
               ::"v"(v0), "v"(v1), "v"(v2), "s"(sbase), "s"(lds) : "memory");
}
#define PEEK_ASYNC(v, a) asm volatile("s_mov_b32 exec_hi, 0\n\tglobal_atomic_add %0, %1, %2, off sc0 sc1\n\ts_mov_b32 exec_hi, -1" : "+v"(v) : "v"(a), "v"(0u) : "memory")
DI void poke(GAS unsigned* a, unsigned v) { asm volatile("global_atomic_swap %0, %1, off sc1" ::"v"(a), "v"(v) : "memory"); }
DI unsigned pack2(f32x2 v) { return __builtin_bit_cast(unsigned, __builtin_convertvector(v, bf16x2)); }
DI int slice_c0(int j) { return 11 * (j >> 2) + 3 * (j & 3) - (j == 15); }
DI int slice_np(int j) { return ((j & 3) == 3 || j == 14) ? 2 : 3; }
template <int N> struct IC { static constexpr int value = N; };
#define MFMA16(a, b, c) __builtin_amdgcn_mfma_f32_16x16x32_bf16((a), (b), (c), 0, 0, 0)
#define TR(a) __builtin_amdgcn_ds_read_tr16_b64_v4i16((LAS s16x4*)(a))
#define CAT(lo, hi) __builtin_shufflevector(lo, hi, 0, 1, 2, 3, 4, 5, 6, 7)
#define F(...) do { __VA_ARGS__; FENCE(); } while (0)

__global__ __launch_bounds__(512, 2) void geglu_kernel(const char* __restrict__ x, const char* __restrict__ wg,
                                                       const char* __restrict__ wu, const char* __restrict__ wd,
                                                       char* __restrict__ out, char* __restrict__ ws) {
  __shared__ __attribute__((aligned(16))) char smem[LDS_BYTES];
  u64 pc0;
  asm volatile("s_getpc_b64 %0" : "=s"(pc0));
  const unsigned lds0 = (unsigned)(size_t)(LAS char*)smem;
  const unsigned tid = __builtin_amdgcn_workitem_id_x();
  const unsigned lane = tid & 63;
  const int w = __builtin_amdgcn_readfirstlane(tid >> 6);
  int b = __builtin_amdgcn_workgroup_id_x();
  const unsigned rip = lane >> 3, pc = lane & 7;
  const int p_ = (b & 4) * 2 + (b >> 5), rot_ = (b & 4) + ((b + p_) & 3);
  const unsigned ve = (p_ * 128 + (w & 3) * 32 + rip) * 1024 + ((pc ^ (rip >> 1)) << 4), vo = ve ^ 64;
  unsigned v0 = ve, v1 = vo + 7168, v2 = ve + 2 * 7168, v3 = vo + 3 * 7168;
  asm volatile("" : "+v"(v0), "+v"(v1), "+v"(v2), "+v"(v3) : "s"(x), "s"(wg), "s"(wu), "s"(wd), "s"(out), "s"(ws));
  if (__builtin_expect(w < 4, 1)) {
    __builtin_amdgcn_s_setprio(2);
    dma4(v0, v1, v2, v3, x + rot_ * 128, lds0 + w * 4096);
  }
  asm volatile("" : "+s"(b));
  FENCE();
  const int xcd = b & 7, bi = b >> 3;
  const int p = (xcd >> 2) * 8 + (bi >> 2), j = (xcd & 3) * 4 + (bi & 3);
  const int c0 = slice_c0(j), np = slice_np(j);
  const int rt = xcd * 4 + (bi >> 3), nt = bi & 7;
  unsigned pv = 0, ctag = 0;
  GAS unsigned* cbox = nullptr;
  LAS unsigned* arrive = (LAS unsigned*)(size_t)(lds0 + 39 * 4096);
  constexpr unsigned ARRIVED = 0x7fc1a5e3u;
  auto chunk_of = [&](int g) { int c = (g == 43 ? 39 : g) + 5 * xcd; c -= c >= NCH ? NCH : 0; return c; };
  auto issue_wd = [&](int i0, int i1, int step) {
    const unsigned de = rip * 1024 + ((pc ^ (rip & 2)) << 4), dq = de ^ 64;
#pragma unroll 1
    for (int i = i0 + w; i < i1; i += step)
      dma4(de, dq + 7168, de + 2 * 7168, dq + 3 * 7168, wd + (size_t)(unsigned)__builtin_amdgcn_readfirstlane(chunk_of(i) * 32768 + nt * 128), lds0 + (i >= 40 ? i - 40 : i) * 4096);
  };

  if (__builtin_expect(w < 4, 1)) {
    const int rot = (xcd & 4) + ((xcd + p) & 3);
    unsigned v[3];
    v[0] = ((lane >> 2) + (w >> 1) * 16) * WPITCH + (((lane & 3) ^ ((lane >> 4) & 2)) << 4) + c0 * 64 + 2048;
    v[1] = v[0] - 960;
    v[2] = v[0] - (np == 2 ? 1984 : 1920);
    const char* wb = ((w & 1) ? wu : wg) - 2048;
    const unsigned wl0 = 16384 + (w & 1) * 12288 + (w >> 1) * 3072;
    dma3(v[0], v[1], v[2], wb + (size_t)(unsigned)(rot * 64 * WPITCH), lds0 + wl0);
    dma3(v[0], v[1], v[2], wb + (size_t)(unsigned)((rot * 64 + 32) * WPITCH), lds0 + wl0 + 6144);
    FENCE();
#pragma unroll 1
    for (int s = 1; s < 8; ++s) {
      const int kc = rot ^ s;
      const unsigned sl = lds0 + (s & 3) * STAGE;
      const char* wk = wb + (size_t)(unsigned)(kc * 64 * WPITCH);
      dma4(v0, v1, v2, v3, x + kc * 128, sl + w * 4096);
      dma3(v[0], v[1], v[2], wk, sl + wl0);
      if (s == 1 && w == 3) {
        asm volatile("global_load_dword %0, %1, %2 offset:128 sc1" : "+v"(ctag) : "v"((rt * 8 + nt) * MBOX), "s"(ws) : "memory");
      }
      if (s == 2 && (j & 3) == 3) asm volatile("s_mov_b32 exec_hi, 0x7ff\n\tglobal_load_dword %0, %1, %2\n\ts_mov_b32 exec_hi, -1" : "+v"(pv) : "v"((((xcd * 8 + (bi >> 2)) * 4 + w) * NCH + lane) * 128), "s"(wd) : "memory");
      if (s == 2 && (j & 3) == 3 && w == 2 && lane < 8)
        asm volatile("global_atomic_add %0, %1, %2 sc1" ::"v"((xcd * 32 + (bi >> 2) * 4 + (lane >> 1)) * MBOX + (lane & 1) * 64), "v"(0u), "s"(ws) : "memory");
      WAITV(10); BAR();
      dma3(v[0], v[1], v[2], wk + 32 * WPITCH, sl + wl0 + 6144);
      WAITV(10); BAR();
    }
    OPAQUE_V(ctag);
    ctag += TAG_STEP;
    issue_wd(0, 8, 4);
    WAITV(11); BAR();
    issue_wd(8, 12, 4);
    WAITV(12); BAR();
    issue_wd(12, 16, 4);
    BAR();
    BAR();
    __builtin_amdgcn_s_setprio(0);
    issue_wd(16, 20, 4);
  } else {
    const int wm = w & 1, wn = (w >> 1) & 1;
    const int m16 = lane & 15, q4 = lane >> 4, q = m16 >> 2, pp = lane & 3;
    const __amdgpu_buffer_rsrc_t hrs = __builtin_amdgcn_make_buffer_rsrc(ws + WS_H, 0, H_BYTES, 0x00020000);
    cbox = (GAS unsigned*)(ws + ((2 * p + wm) * 8 + (lane & 7)) * MBOX);
    asm volatile("global_load_dword %0, %1, off offset:128 sc1" : "+v"(ctag) : "v"(cbox) : "memory");
    cbox += wn * 16 + j;
    {
      if (w == 5) asm volatile("global_load_dword %0, %1, %2" : "+v"(pv) : "v"((bi * 4 + (lane & 3)) * 128), "s"(pc0 & ~255ull) : "memory");
    }
    auto phase1 = [&](auto npc) {
    constexpr int NP = decltype(npc)::value;
    f32x4 ag[3][4], au[3][4];
#pragma unroll
    for (int t = 0; t < (NP == 3 ? 0 : NP); ++t)
#pragma unroll
      for (int m = 0; m < 4; ++m) { ag[t][m] = (f32x4)(0.f); au[t][m] = (f32x4)(0.f); asm volatile("" : "+v"(ag[t][m]), "+v"(au[t][m])); }
    const unsigned xa0 = lds0 + (wm * 64 + m16) * 128 + ((q4 ^ (m16 >> 1)) << 4);
    const int t0 = wn * NP;
    const unsigned wrow = lds0 + 16384 + (q4 >> 1) * 3072 + ((q4 & 1) * 8 + q) * 64 + pp * 8;
    const unsigned waA = wrow + (t0 >> 1) * 1024 + (((t0 ^ q4) & 1) << 5);
    const unsigned waB = wrow + ((t0 + 1) >> 1) * 1024 + ((((t0 + 1) ^ q4) & 1) << 5);
    s16x8 xf[2][4];
    s16x4 wl[6], wh[6];
#define WOFF(i) (((i) >> 2) * 1024 + ((i) & 1) * 12288)
#define RDL(A, B, i) wl[i] = TR((((i) >> 1) == 1 ? B : A) + WOFF(i))
#define RDH(A, B, i) wh[i] = TR((((i) >> 1) == 1 ? B : A) + WOFF(i) + 256)
#define RDX(X, b, m) xf[b][m] = *(LAS s16x8*)((X) + (m) * 2048)
#define MM(b, i, m) do { if ((i) & 1) au[(i) >> 1][m] = MFMA16(CAT(wl[i], wh[i]), xf[b][m], au[(i) >> 1][m]); else ag[(i) >> 1][m] = MFMA16(CAT(wl[i], wh[i]), xf[b][m], ag[(i) >> 1][m]); FENCE(); } while (0)
#define ACC(i, m) (((i) & 1) ? au[(i) >> 1][m] : ag[(i) >> 1][m])
#define MA(n, i, m) do { \
      if ((n) == 0) asm volatile("v_mfma_f32_16x16x32_bf16 %0, %1, %2, 0" : "=&v"(ACC(i, m)) : "v"(CAT(wl[i], wh[i])), "v"(xf[(n) & 1][m])); \
      else asm volatile("v_mfma_f32_16x16x32_bf16 %0, %1, %2, %0" : "+v"(ACC(i, m)) : "v"(CAT(wl[i], wh[i])), "v"(xf[(n) & 1][m])); \
      FENCE(); } while (0)
#define USET(n) (((n) >> 2) & 1)
#define UOFF(n) ((((n) >> 1) & 1) * STAGE)
#define UL(n, i) do { if ((n) < 16) F(RDL(bA[USET(n)] + UOFF(n) + ((n) & 1) * 6144, bB[USET(n)] + UOFF(n) + ((n) & 1) * 6144, i)); } while (0)
#define UH(n, i) do { if ((n) < 16) F(RDH(bA[USET(n)] + UOFF(n) + ((n) & 1) * 6144, bB[USET(n)] + UOFF(n) + ((n) & 1) * 6144, i)); } while (0)
#define UX(n, m) do { if ((n) < 16) F(RDX(bX[USET(n)][(n) & 1] + UOFF(n), (n) & 1, m)); } while (0)
#define UNIT(n) \
      MA(n, 0, 0); UL(n, 3); MA(n, 0, 1); UH(n, 3); MA(n, 0, 2); MA(n, 0, 3); \
      MA(n, 1, 0); UL(n, 4); MA(n, 1, 1); UH(n, 4); MA(n, 1, 2); MA(n, 1, 3); \
      MA(n, 2, 0); UL(n, 5); MA(n, 2, 1); UH(n, 5); MA(n, 2, 2); F(BAR()); \
      MA(n, 2, 3); UL(n + 1, 0); MA(n, 3, 0); UH(n + 1, 0); MA(n, 3, 1); UX(n + 1, 0); MA(n, 3, 2); UX(n + 1, 1); \
      MA(n, 3, 3); UX(n + 1, 2); MA(n, 4, 0); UX(n + 1, 3); MA(n, 4, 1); UL(n + 1, 1); MA(n, 4, 2); UH(n + 1, 1); \
      MA(n, 4, 3); MA(n, 5, 0); UL(n + 1, 2); MA(n, 5, 1); UH(n + 1, 2); MA(n, 5, 2); MA(n, 5, 3);
    unsigned bA[2] = {waA, waA + 2 * STAGE}, bB[2] = {waB, waB + 2 * STAGE}, bX[2][2] = {{xa0, xa0 ^ 64}, {xa0 + 2 * STAGE, (xa0 ^ 64) + 2 * STAGE}};
    if constexpr (NP == 3) asm volatile("" : "+v"(bA[0]), "+v"(bA[1]), "+v"(bB[0]), "+v"(bB[1]), "+v"(bX[0][0]), "+v"(bX[0][1]), "+v"(bX[1][0]), "+v"(bX[1][1]));
#define STEP2(b, CA, CB, NX, NA, NB, G0, G1, G2, G3) \
      F(G0); F(G1); F(G2); F(G3); \
      MM(b, 0, 0); F(RDL(CA, CB, 3)); MM(b, 0, 1); F(RDH(CA, CB, 3)); MM(b, 0, 2); MM(b, 0, 3); F(BAR()); \
      MM(b, 1, 0); F(RDL(NA, NB, 0)); MM(b, 1, 1); F(RDH(NA, NB, 0)); MM(b, 1, 2); F(RDX(NX, b ^ 1, 0)); MM(b, 1, 3); F(RDX(NX, b ^ 1, 1)); \
      MM(b, 2, 0); F(RDX(NX, b ^ 1, 2)); MM(b, 2, 1); F(RDX(NX, b ^ 1, 3)); MM(b, 2, 2); F(RDL(NA, NB, 1)); MM(b, 2, 3); F(RDH(NA, NB, 1)); \
      MM(b, 3, 0); F(RDL(NA, NB, 2)); MM(b, 3, 1); F(RDH(NA, NB, 2)); MM(b, 3, 2); MM(b, 3, 3);
    int hv = (q4 & 1) * 1024 + (q4 >> 1) * 256 + m16 * 16;
    int hso[2 * NP];
#pragma unroll
    for (int u = 0; u < 2 * NP; ++u) { hso[u] = ((p * 4 + wm * 2 + (u & 1)) * NCH + c0 + ((t0 + (u >> 1)) >> 1)) * 2048 + ((t0 + (u >> 1)) & 1) * 512; asm volatile("" : "+s"(hso[u])); }
    OPAQUE_V(hv);
    FENCE();
    __builtin_amdgcn_s_setprio(3);
    BAR();
    RDL(waA, waB, 0); RDH(waA, waB, 0); RDX(xa0, 0, 0); RDX(xa0, 0, 1); RDX(xa0, 0, 2); RDX(xa0, 0, 3);
    RDL(waA, waB, 1); RDH(waA, waB, 1); RDL(waA, waB, 2); RDH(waA, waB, 2);
    unsigned ca = waA, cb = waB, cx = xa0 ^ 64, na, nb, nx;
    unsigned sn = 0;
    FENCE();
    if constexpr (NP == 3) {
      UNIT(0) UNIT(1) UNIT(2) UNIT(3) UNIT(4) UNIT(5) UNIT(6) UNIT(7) UNIT(8) UNIT(9) UNIT(10) UNIT(11) UNIT(12) UNIT(13) UNIT(14) UNIT(15)
      asm volatile("s_nop 7" ::: "memory");
      FENCE();
    } else {
#pragma unroll 1
      for (int s = 0; s < 8; ++s) {
        STEP2(0, ca, cb, cx, ca + 6144, cb + 6144, sn = sn == 3 * STAGE ? 0 : sn + STAGE, na = waA + sn, nb = waB + sn, nx = xa0 + sn)
        STEP2(1, ca + 6144, cb + 6144, nx, na, nb, cx = nx ^ 64, (void)0, (void)0, (void)0)
        ca = na; cb = nb;
      }
    }
    BAR();
    constexpr float K0 = -2.0f * 0.7978845608028654f * 1.4426950408889634f;
    constexpr float K1 = K0 * 0.044715f;
    f32x2 z[2][4];
    unsigned hp[4];
#define PR(A, u, i) (f32x2){A[(u) >> 1][2 * ((u) & 1) + ((i) >> 1)][((i) & 1) * 2], A[(u) >> 1][2 * ((u) & 1) + ((i) >> 1)][((i) & 1) * 2 + 1]}
#define A1(u, i) F(z[(u) & 1][i] = PR(ag, u, i) * PR(ag, u, i));
#define A2(u, i) F(z[(u) & 1][i] = __builtin_elementwise_fma(z[(u) & 1][i], (f32x2)(K1), (f32x2)(K0)));
#define A3(u, i) F(z[(u) & 1][i] *= PR(ag, u, i));
#define EX(u, n) F(z[(u) & 1][(n) >> 1][(n) & 1] = __builtin_amdgcn_exp2f(z[(u) & 1][(n) >> 1][(n) & 1]));
#define AD(u, i) F(z[(u) & 1][i] += 1.0f);
#define RC(u, n) F(z[(u) & 1][(n) >> 1][(n) & 1] = __builtin_amdgcn_rcpf(z[(u) & 1][(n) >> 1][(n) & 1]));
#define MU(u, i) F(z[(u) & 1][i] *= PR(au, u, i));
#define CV(u, i) F(hp[i] = pack2(z[(u) & 1][i]));
#define HST(u) { \
      const auto s0 = __builtin_amdgcn_permlane16_swap(hp[0], hp[2], false, false); \
      const auto s1 = __builtin_amdgcn_permlane16_swap(hp[1], hp[3], false, false); \
      const u32x4 v = {s0[0], s1[0], s0[1], s1[1]}; \
      __builtin_amdgcn_raw_buffer_store_b128(v, hrs, hv, hso[u], 16); \
      FENCE(); \
      asm volatile("s_nop 1" ::"v"(v));   \
      FENCE(); }
    FENCE();
    if constexpr (NP == 3) {
      A1(0, 0) A1(0, 1) A1(0, 2) A1(0, 3) A2(0, 0) A2(0, 1) A2(0, 2) A2(0, 3) A3(0, 0) A3(0, 1) A3(0, 2) A3(0, 3) EX(0, 0) EX(0, 1) EX(0, 2) EX(0, 3)
      AD(0, 0) EX(0, 4) EX(0, 5) AD(0, 1) EX(0, 6) EX(0, 7) AD(0, 2) RC(0, 0) AD(0, 3) RC(0, 1) A1(1, 0) A1(1, 1) RC(0, 2) A1(1, 2) A1(1, 3) RC(0, 3)
      A2(1, 0) A2(1, 1) RC(0, 4) A2(1, 2) RC(0, 5) A2(1, 3) A3(1, 0) RC(0, 6) A3(1, 1) A3(1, 2) RC(0, 7) A3(1, 3) EX(1, 0) A3(0, 0) A3(0, 1) EX(1, 1)
      A3(0, 2) A3(0, 3) EX(1, 2) MU(0, 0) MU(0, 1) EX(1, 3) MU(0, 2) MU(0, 3) EX(1, 4) CV(0, 0) CV(0, 1) EX(1, 5) CV(0, 2) CV(0, 3) EX(1, 6) HST(0)
      AD(1, 0) EX(1, 7) AD(1, 1) AD(1, 2) RC(1, 0) AD(1, 3) RC(1, 1) A1(2, 0) A1(2, 1) RC(1, 2) A1(2, 2) A1(2, 3) RC(1, 3) A2(2, 0) A2(2, 1) RC(1, 4)
      A2(2, 2) RC(1, 5) A2(2, 3) A3(2, 0) RC(1, 6) A3(2, 1) A3(2, 2) RC(1, 7) A3(2, 3) EX(2, 0) A3(1, 0) A3(1, 1) EX(2, 1) A3(1, 2) A3(1, 3) EX(2, 2)
      MU(1, 0) MU(1, 1) EX(2, 3) MU(1, 2) MU(1, 3) EX(2, 4) CV(1, 0) CV(1, 1) EX(2, 5) CV(1, 2) CV(1, 3) EX(2, 6) HST(1) AD(2, 0) EX(2, 7) AD(2, 1)
      AD(2, 2) RC(2, 0) AD(2, 3) RC(2, 1) A1(3, 0) A1(3, 1) RC(2, 2) A1(3, 2) A1(3, 3) RC(2, 3) A2(3, 0) A2(3, 1) RC(2, 4) A2(3, 2) RC(2, 5) A2(3, 3)
      A3(3, 0) RC(2, 6) A3(3, 1) A3(3, 2) RC(2, 7) A3(3, 3) EX(3, 0) A3(2, 0) A3(2, 1) EX(3, 1) A3(2, 2) A3(2, 3) EX(3, 2) MU(2, 0) MU(2, 1) EX(3, 3)
      MU(2, 2) MU(2, 3) EX(3, 4) CV(2, 0) CV(2, 1) EX(3, 5) CV(2, 2) CV(2, 3) EX(3, 6) HST(2) AD(3, 0) EX(3, 7) AD(3, 1) AD(3, 2) RC(3, 0) AD(3, 3)
      RC(3, 1) A1(4, 0) A1(4, 1) RC(3, 2) A1(4, 2) A1(4, 3) RC(3, 3) A2(4, 0) A2(4, 1) RC(3, 4) A2(4, 2) RC(3, 5) A2(4, 3) A3(4, 0) RC(3, 6) A3(4, 1)
      A3(4, 2) RC(3, 7) A3(4, 3) EX(4, 0) A3(3, 0) A3(3, 1) EX(4, 1) A3(3, 2) A3(3, 3) EX(4, 2) MU(3, 0) MU(3, 1) EX(4, 3) MU(3, 2) MU(3, 3) EX(4, 4)
      CV(3, 0) CV(3, 1) EX(4, 5) CV(3, 2) CV(3, 3) EX(4, 6) HST(3) AD(4, 0) EX(4, 7) AD(4, 1) AD(4, 2) RC(4, 0) AD(4, 3) RC(4, 1) A1(5, 0) A1(5, 1)
      RC(4, 2) A1(5, 2) A1(5, 3) RC(4, 3) A2(5, 0) A2(5, 1) RC(4, 4) A2(5, 2) RC(4, 5) A2(5, 3) A3(5, 0) RC(4, 6) A3(5, 1) A3(5, 2) RC(4, 7) A3(5, 3)
      EX(5, 0) A3(4, 0) A3(4, 1) EX(5, 1) A3(4, 2) A3(4, 3) EX(5, 2) MU(4, 0) MU(4, 1) EX(5, 3) MU(4, 2) MU(4, 3) EX(5, 4) CV(4, 0) CV(4, 1) EX(5, 5)
      CV(4, 2) CV(4, 3) EX(5, 6) HST(4) AD(5, 0) EX(5, 7) AD(5, 1) AD(5, 2) RC(5, 0) AD(5, 3) RC(5, 1) RC(5, 2) RC(5, 3) RC(5, 4) RC(5, 5) RC(5, 6)
      RC(5, 7) A3(5, 0) A3(5, 1) A3(5, 2) A3(5, 3) MU(5, 0) MU(5, 1) MU(5, 2) MU(5, 3) CV(5, 0) CV(5, 1) CV(5, 2) CV(5, 3) HST(5)
    } else {
      A1(0, 0) A1(0, 1) A1(0, 2) A1(0, 3) A2(0, 0) A2(0, 1) A2(0, 2) A2(0, 3) A3(0, 0) A3(0, 1) A3(0, 2) A3(0, 3) EX(0, 0) EX(0, 1) EX(0, 2) EX(0, 3)
      AD(0, 0) EX(0, 4) EX(0, 5) AD(0, 1) EX(0, 6) EX(0, 7) AD(0, 2) RC(0, 0) AD(0, 3) RC(0, 1) A1(1, 0) A1(1, 1) RC(0, 2) A1(1, 2) A1(1, 3) RC(0, 3)
      A2(1, 0) A2(1, 1) RC(0, 4) A2(1, 2) RC(0, 5) A2(1, 3) A3(1, 0) RC(0, 6) A3(1, 1) A3(1, 2) RC(0, 7) A3(1, 3) EX(1, 0) A3(0, 0) A3(0, 1) EX(1, 1)
      A3(0, 2) A3(0, 3) EX(1, 2) MU(0, 0) MU(0, 1) EX(1, 3) MU(0, 2) MU(0, 3) EX(1, 4) CV(0, 0) CV(0, 1) EX(1, 5) CV(0, 2) CV(0, 3) EX(1, 6) HST(0)
      AD(1, 0) EX(1, 7) AD(1, 1) AD(1, 2) RC(1, 0) AD(1, 3) RC(1, 1) A1(2, 0) A1(2, 1) RC(1, 2) A1(2, 2) A1(2, 3) RC(1, 3) A2(2, 0) A2(2, 1) RC(1, 4)
      A2(2, 2) RC(1, 5) A2(2, 3) A3(2, 0) RC(1, 6) A3(2, 1) A3(2, 2) RC(1, 7) A3(2, 3) EX(2, 0) A3(1, 0) A3(1, 1) EX(2, 1) A3(1, 2) A3(1, 3) EX(2, 2)
      MU(1, 0) MU(1, 1) EX(2, 3) MU(1, 2) MU(1, 3) EX(2, 4) CV(1, 0) CV(1, 1) EX(2, 5) CV(1, 2) CV(1, 3) EX(2, 6) HST(1) AD(2, 0) EX(2, 7) AD(2, 1)
      AD(2, 2) RC(2, 0) AD(2, 3) RC(2, 1) A1(3, 0) A1(3, 1) RC(2, 2) A1(3, 2) A1(3, 3) RC(2, 3) A2(3, 0) A2(3, 1) RC(2, 4) A2(3, 2) RC(2, 5) A2(3, 3)
      A3(3, 0) RC(2, 6) A3(3, 1) A3(3, 2) RC(2, 7) A3(3, 3) EX(3, 0) A3(2, 0) A3(2, 1) EX(3, 1) A3(2, 2) A3(2, 3) EX(3, 2) MU(2, 0) MU(2, 1) EX(3, 3)
      MU(2, 2) MU(2, 3) EX(3, 4) CV(2, 0) CV(2, 1) EX(3, 5) CV(2, 2) CV(2, 3) EX(3, 6) HST(2) AD(3, 0) EX(3, 7) AD(3, 1) AD(3, 2) RC(3, 0) AD(3, 3)
      RC(3, 1) RC(3, 2) RC(3, 3) RC(3, 4) RC(3, 5) RC(3, 6) RC(3, 7) A3(3, 0) A3(3, 1) A3(3, 2) A3(3, 3) MU(3, 0) MU(3, 1) MU(3, 2) MU(3, 3) CV(3, 0)
      CV(3, 1) CV(3, 2) CV(3, 3) HST(3)
    }
    };
    if (np == 3) phase1(IC<3>{}); else phase1(IC<2>{});
    WAITV(0);
    OPAQUE_V(ctag); OPAQUE_V(pv);
    if (lane < 8) poke(cbox, ctag + TAG_STEP);
    arrive[w] = ARRIVED;
    __builtin_amdgcn_s_setprio(0);
    FENCE();
  }

  const int mh = w & 1, kq = w >> 1;
  unsigned ln = lane;
  OPAQUE_V(ln);
  const int m16 = ln & 15, q4 = ln >> 4, q = m16 >> 2, pp = ln & 3;
  const __amdgpu_buffer_rsrc_t hrs = __builtin_amdgcn_make_buffer_rsrc(ws + WS_H, 0, H_BYTES, 0x00020000);
  GAS unsigned* mybox = (GAS unsigned*)(ws + (rt * 8 + nt) * MBOX) + ln;
  unsigned da[3][4];
  {
    const unsigned fsw = (q >> 1) | ((q4 & 1) << 1);
    const unsigned drow = lds0 + kq * 4096 + (8 * q4 + q) * 128 + pp * 8;
#pragma unroll
    for (int t = 0; t < 4; ++t)
#pragma unroll
      for (int g = 0; g < 3; ++g) { da[g][t] = drow + (((t ^ (kq & 2)) ^ fsw) << 5) + g * 65536; OPAQUE_V(da[g][t]); }
  }
  s16x4 af[11][8];
#define RA(i, r) af[i][r] = TR(da[((i) % 10) >> 2][(r) >> 1] + (((i) % 10) & 3) * 16384 + ((r) & 1) * 512)
  int so[11];
#pragma unroll
  for (int i = 0; i < 11; ++i) {
    so[i] = (i == 9 && kq == 3) ? H_BYTES : ((2 * rt + mh) * NCH + chunk_of(4 * i + kq)) * 2048;
    asm volatile("" : "+s"(so[i]));
  }
  f32x4 o[4][2];
#pragma unroll
  for (int t = 0; t < 4; ++t) { o[t][0] = (f32x4)(0.f); o[t][1] = (f32x4)(0.f); OPAQUE_V(o[t][0]); OPAQUE_V(o[t][1]); }
  int hvo[2] = {(int)ln * 16 + (kq & 1) * 1024, (int)ln * 16 + ((kq & 1) ^ 1) * 1024};
  OPAQUE_V(hvo[0]); OPAQUE_V(hvo[1]);
  unsigned xw[3];
#pragma unroll
  for (int d = 0; d < 3; ++d) { xw[d] = lds0 + 16384 + mh * 24576 + (kq ^ (d + 1)) * 6144 + d * 2048 + ln * 16; OPAQUE_V(xw[d]); }
  unsigned xr = lds0 + 16384 + mh * 24576 + kq * 6144 + ln * 16;
  int oo = (rt * 64 + mh * 32 + (kq & 1) * 16 + m16) * 1024 + nt * 128 + (kq >> 1) * 64 + (q4 & 1) * 32 + (q4 >> 1) * 16;
  OPAQUE_V(xr); OPAQUE_V(oo);
  FENCE();
  if (w < 4) {
#pragma unroll 1
    for (unsigned spin = 0; spin < SPIN_CAP; ++spin) {
      if (__all(arrive[4 + (lane & 3)] == ARRIVED)) break;
      asm volatile("s_sleep 1" ::: "memory");
    }
    WAITV(0);
    OPAQUE_V(pv);
    if (w < 3) issue_wd(20, 39, 3);
  }
  if (w == 3) {
    pv = ctag + 1;
    PEEK_ASYNC(pv, mybox);
#pragma unroll 1
    for (unsigned spin = 0; spin < SPIN_CAP; ++spin) {
      __builtin_amdgcn_s_sleep(4);
      PEEK_ASYNC(pv, mybox);
      WAITV(1); OPAQUE_V(pv);
      if (__all(lane >= 32 || pv == ctag)) break;
    }
  }
  FENCE();
  BAR();
  u32x4 hf[11][2];
#define L(i, e) F(hf[i][e] = __builtin_amdgcn_raw_buffer_load_b128(hrs, hvo[e], so[i], 16))
#define R8(i) do { RA(i, 0); RA(i, 1); RA(i, 2); RA(i, 3); RA(i, 4); RA(i, 5); RA(i, 6); RA(i, 7); FENCE(); } while (0)
  L(0, 0); R8(0); L(0, 1); R8(1); L(1, 0); R8(2); L(1, 1); R8(3); L(2, 0); R8(4); L(2, 1); L(3, 0); L(3, 1);
#define M2(i, t, e) F(asm volatile("v_mfma_f32_16x16x32_bf16 %0, %1, %2, %0" : "+v"(o[t][e]) : "v"(CAT(af[i][2 * (t)], af[i][2 * (t) + 1])), "v"(hf[i][e])))
#define NOP (void)0
#define RR(n, r) F(RA(n, r))
#define RN(n, r) NOP
#define ITER(i, LA, LB, RD, n) \
  M2(i, 0, 0); RD(n, 0); M2(i, 1, 0); RD(n, 1); M2(i, 2, 0); RD(n, 2); M2(i, 3, 0); RD(n, 3); LA; \
  M2(i, 0, 1); RD(n, 4); M2(i, 1, 1); RD(n, 5); M2(i, 2, 1); RD(n, 6); M2(i, 3, 1); RD(n, 7); LB;
  ITER(0, L(4, 0), L(4, 1), RN, 0)
  ITER(1, L(5, 0), L(5, 1), RN, 0)
  OPAQUE_V(pv);
  F(BAR());
  if (w < 4) issue_wd(40, 44, 4);
  ITER(2, L(6, 0), L(6, 1), RR, 5)
  ITER(3, L(7, 0), L(7, 1), RR, 6)
  ITER(4, L(8, 0), L(8, 1), RR, 7)
  ITER(5, L(9, 0), L(9, 1), RR, 8)
  ITER(6, L(10, 0), L(10, 1), RR, 9)
  F(BAR());
  ITER(7, NOP, NOP, RR, 10)
  ITER(8, NOP, NOP, RN, 0)
  if (kq != 3) { ITER(9, NOP, NOP, RN, 0) }
  ITER(10, NOP, NOP, RN, 0)
  asm volatile("s_nop 7\n\ts_nop 1" : "+v"(o[0][0]), "+v"(o[1][0]), "+v"(o[2][0]), "+v"(o[3][0]), "+v"(o[0][1]), "+v"(o[1][1]), "+v"(o[2][1]), "+v"(o[3][1]));
  {
#pragma unroll
    for (int d = 0; d < 3; ++d) { *(LAS f32x4*)(size_t)xw[d] = o[(d + 1) & 2][(d + 1) & 1]; *(LAS f32x4*)(size_t)(xw[d] + 1024) = o[((d + 1) & 2) + 1][(d + 1) & 1]; }
    BAR_L();
    f32x4 r0[3], r1[3];
#pragma unroll
    for (int d = 0; d < 3; ++d) { r0[d] = *(LAS f32x4*)(size_t)(xr + d * 2048); r1[d] = *(LAS f32x4*)(size_t)(xr + d * 2048 + 1024); }
    const f32x4 s0 = (o[0][0] + r0[0]) + (r0[1] + r0[2]);
    const f32x4 s1 = (o[1][0] + r1[0]) + (r1[1] + r1[2]);
    const auto e0 = __builtin_amdgcn_permlane16_swap(pack2((f32x2){s0[0], s0[1]}), pack2((f32x2){s1[0], s1[1]}), false, false);
    const auto e1 = __builtin_amdgcn_permlane16_swap(pack2((f32x2){s0[2], s0[3]}), pack2((f32x2){s1[2], s1[3]}), false, false);
    const u32x4 v = {e0[0], e1[0], e0[1], e1[1]};
    const __amdgpu_buffer_rsrc_t ors = __builtin_amdgcn_make_buffer_rsrc(out, 0, S * DM * 2, 0x00020000);
    __builtin_amdgcn_raw_buffer_store_b128(v, ors, oo, 0, 16);
  }
  if (w == 3 && lane == 32) poke(mybox, ctag);
}

extern "C" void kernel_launch(void* const* d_in, const int* in_sizes, int n_in, void* d_out, int out_size, void* d_ws,
                              size_t ws_size, hipStream_t stream) {
  geglu_kernel<<<256, 512, 0, stream>>>((const char*)d_in[0], (const char*)d_in[1], (const char*)d_in[2],
                                        (const char*)d_in[3], (char*)d_out, (char*)d_ws);
}
```
